```python
import torch

def _softmax(x, axis=-1):
    return torch.softmax(x, dim=axis)
def _gelu(x):
    return torch.nn.functional.gelu(x, approximate="tanh")
def _silu(x):
    return torch.nn.functional.silu(x)
def _sigmoid(x):
    return torch.sigmoid(x)
def _layernorm(x, axis=-1, eps=1e-5):
    m = x.mean(dim=axis, keepdim=True)
    v = x.var(dim=axis, keepdim=True, unbiased=False)
    return (x - m) / torch.sqrt(v + eps)
def _scatter_add(tgt, idx, src, axis=0):
    return tgt.clone().index_add_(axis, idx, src)
def _segment_sum(src, seg, n):
    out = torch.zeros((n,) + src.shape[1:], dtype=src.dtype)
    return out.index_add_(0, seg, src)
def _pad(x, pw):
    flat = tuple(v for pair in reversed(pw) for v in pair)
    return torch.nn.functional.pad(x, flat)

kernel_name = 'gated_mix__grouped_kv_mqa__ssm_conv_s5__prompt'

def setup_inputs():

    _w = (lambda a: (lambda t: t.to(torch.bfloat16) if t.is_floating_point() else t)(torch.as_tensor(a)))
    def _A_setup():
        B, S, D, H = 1, 2048, 448, 16
        Dh = D // H
        return {
            "x": _w((lambda *s: torch.randn(*s, dtype=torch.bfloat16) * 0.05)(B, S, D)),
            "wq": _w((lambda *s: torch.randn(*s, dtype=torch.bfloat16) * 1.6)(D, H * Dh)),
            "wk": _w((lambda *s: torch.randn(*s, dtype=torch.bfloat16) * 1.6)(D, Dh)),
            "wv": _w((lambda *s: torch.randn(*s, dtype=torch.bfloat16) * 0.05)(D, Dh)),
            "wo": _w((lambda *s: torch.randn(*s, dtype=torch.bfloat16) * 0.05)(H * Dh, D)),
        }
    _A_in = _A_setup()

    def _B_setup():
        B, S, D = 1, 2048, 448
        P = D
        Lambda_re = -0.5 * (lambda _x: torch.exp(torch.as_tensor(_x)))((lambda *s: torch.randn(*s, dtype=torch.bfloat16) * 0.4)(P))
        Lambda_im = (lambda *s: torch.randn(*s, dtype=torch.bfloat16) * 0.8)(P) * 1.0
        B_re = (lambda *s: torch.randn(*s, dtype=torch.bfloat16) * 0.4)(P, D) * (D ** -0.5)
        B_im = (lambda *s: torch.randn(*s, dtype=torch.bfloat16) * 0.4)(P, D) * (D ** -0.5)
        C_re = (lambda *s: torch.randn(*s, dtype=torch.bfloat16) * 0.4)(D, P) * (P ** -0.5)
        C_im = (lambda *s: torch.randn(*s, dtype=torch.bfloat16) * 0.4)(D, P) * (P ** -0.5)
        log_dt = (lambda *s: torch.randn(*s, dtype=torch.bfloat16) * 0.4)(P) * 0.3 - 2.0
        Dskip = (lambda *s: torch.randn(*s, dtype=torch.bfloat16) * 0.2)(D)
        return {
            "x": _w((lambda *s: torch.randn(*s, dtype=torch.bfloat16) * 0.05)(B, S, D)),
            "Lambda_re": _w(Lambda_re),
            "Lambda_im": _w(Lambda_im),
            "B_re": _w(B_re),
            "B_im": _w(B_im),
            "C_re": _w(C_re),
            "C_im": _w(C_im),
            "log_dt": _w(log_dt),
            "Dskip": _w(Dskip),
        }
    _B_in = _B_setup()

    return {
        "A_x": _A_in["x"],
        "A_wq": _A_in["wq"],
        "A_wk": _A_in["wk"],
        "A_wv": _A_in["wv"],
        "A_wo": _A_in["wo"],
        "B_x": _B_in["x"],
        "B_Lambda_re": _B_in["Lambda_re"],
        "B_Lambda_im": _B_in["Lambda_im"],
        "B_B_re": _B_in["B_re"],
        "B_B_im": _B_in["B_im"],
        "B_C_re": _B_in["C_re"],
        "B_C_im": _B_in["C_im"],
        "B_log_dt": _B_in["log_dt"],
        "B_Dskip": _B_in["Dskip"],
        "gate_w": _w((lambda *s: torch.randn(*s, dtype=torch.bfloat16) * 0.05)(448) * 16.0),
    }

def reference(A_x, A_wq, A_wk, A_wv, A_wo, B_x, B_Lambda_re, B_Lambda_im, B_B_re, B_B_im, B_C_re, B_C_im, B_log_dt, B_Dskip, gate_w):
    def _A_ref(x, wq, wk, wv, wo):
        B, S, D = x.shape
        H = 16
        Dh = D // H
        q = (lambda x, *d: x.permute(*d))((x @ wq).reshape(B, S, H, Dh), 0, 2, 1, 3)
        k = x @ wk
        v = x @ wv
        scores = torch.einsum("bhqd,bkd->bhqk", q, k) * (Dh ** -0.5)
        p = _softmax(scores, axis=-1)
        o = torch.einsum("bhqk,bkd->bhqd", p, v)
        return (lambda x, *d: x.permute(*d))(o, 0, 2, 1, 3).reshape(B, S, H * Dh) @ wo

    def _B_ref(x, Lambda_re, Lambda_im, B_re, B_im, C_re, C_im, log_dt, Dskip):
        Bsz, L, H = x.shape
        Lam = Lambda_re + 1j * Lambda_im
        Bmat = B_re + 1j * B_im
        Cmat = C_re + 1j * C_im
        dt = torch.exp(log_dt)
        Lam_bar = torch.exp(dt * Lam)
        B_bar = ((Lam_bar - 1.0) / Lam)[:, None] * Bmat
        Bu = torch.einsum("bld,pd->blp", x + 0j, B_bar)
        h = torch.zeros_like(Bu[:, 0, :])
        hs = []
        for t in range(L):
            h = Lam_bar[None, :] * h + Bu[:, t, :]
            hs.append(h)
        hseq = torch.stack(hs, 1)
        y = 2.0 * torch.einsum("blp,dp->bld", hseq, Cmat).real + x * Dskip[None, None, :]
        return y

    x = A_x
    g = _sigmoid((x * gate_w).sum(-1, keepdims=True))
    a = _A_ref(x, A_wq, A_wk, A_wv, A_wo)
    b = _B_ref(x, B_Lambda_re, B_Lambda_im, B_B_re, B_B_im, B_C_re, B_C_im, B_log_dt, B_Dskip)
    return g * a + (1 - g) * b
```

```cpp
#include <hip/hip_runtime.h>
#include <hip/hip_bf16.h>
#include <stdint.h>
typedef unsigned short u16;
#define S_ 2048
#define D_ 448
#define H_ 16
#define DH 28
__device__ __forceinline__ float b2f(u16 v) { return __uint_as_float(((unsigned)v) << 16); }
__device__ __forceinline__ u16 f2b(float f) { unsigned u = __float_as_uint(f); u += 0x7fff + ((u >> 16) & 1); return (u16)(u >> 16); }
#define NP 1408
__global__ void k_proj(const u16* x, const u16* wq, const u16* wk, const u16* wv, const u16* bre, const u16* bim, const u16* gw, float* out) {
  __shared__ float xs[16][D_];
  int t0 = blockIdx.x * 16;
  for (int i = threadIdx.x; i < 16 * D_; i += 256) xs[i / D_][i % D_] = b2f(x[(size_t)t0 * D_ + i]);
  __syncthreads();
  int n = blockIdx.y * 256 + threadIdx.x;
  if (n >= 1401) return;
  const u16* p; int st;
  if (n < 448) { p = wq + n; st = 448; } else if (n < 476) { p = wk + (n - 448); st = 28; } else if (n < 504) { p = wv + (n - 476); st = 28; }
  else if (n < 952) { p = bre + (size_t)(n - 504) * D_; st = 1; } else if (n < 1400) { p = bim + (size_t)(n - 952) * D_; st = 1; } else { p = gw; st = 1; }
  float acc[16];
#pragma unroll
  for (int i = 0; i < 16; ++i) acc[i] = 0.f;
  for (int k = 0; k < D_; ++k) { float w = b2f(p[(size_t)k * st]);
#pragma unroll
    for (int i = 0; i < 16; ++i) acc[i] += xs[i][k] * w; }
#pragma unroll
  for (int i = 0; i < 16; ++i) out[(size_t)(t0 + i) * NP + n] = acc[i];
}
__global__ void k_scan(const float* pr, const u16* lre, const u16* lim, const u16* ldt, float* h) {
  int p = blockIdx.x * 64 + threadIdx.x;
  float a = b2f(lre[p]), b = b2f(lim[p]); float dt = b2f(f2b(expf(b2f(ldt[p]))));
  float m = expf(dt * a), sn, cs; sincosf(dt * b, &sn, &cs); float lr = m * cs, li = m * sn;
  float nr = lr - 1.f, ni = li, den = a * a + b * b; float cr = (nr * a + ni * b) / den, ci = (ni * a - nr * b) / den;
  float hr = 0.f, hi = 0.f;
  for (int t = 0; t < S_; ++t) { float ur = pr[(size_t)t * NP + 504 + p], ui = pr[(size_t)t * NP + 952 + p];
    float br = cr * ur - ci * ui, bi = cr * ui + ci * ur;
    float nhr = lr * hr - li * hi + br, nhi = lr * hi + li * hr + bi; hr = nhr; hi = nhi;
    h[(size_t)t * 896 + p] = b2f(f2b(hr)); h[(size_t)t * 896 + 448 + p] = b2f(f2b(hi)); }
}
__global__ void k_attn(const float* pr, float* o) {
  __shared__ float ks[64][DH], vs[64][DH];
  int idx = blockIdx.x * 64 + threadIdx.x; int t = idx / H_, hh = idx % H_;
  float q[DH], acc[DH]; const float c = 0.18898223650461362f * 1.4426950408889634f;
#pragma unroll
  for (int d = 0; d < DH; ++d) { q[d] = b2f(f2b(pr[(size_t)t * NP + hh * DH + d] * c)); acc[d] = 0.f; }
  float l = 0.f, m = -1e30f;
  for (int j0 = 0; j0 < S_; j0 += 64) { __syncthreads();
    for (int i = threadIdx.x; i < 64 * DH; i += 64) { ks[i / DH][i % DH] = b2f(f2b(pr[(size_t)(j0 + i / DH) * NP + 448 + i % DH])); vs[i / DH][i % DH] = b2f(f2b(pr[(size_t)(j0 + i / DH) * NP + 476 + i % DH])); }
    __syncthreads();
    for (int j = 0; j < 64; ++j) { float s = 0.f;
#pragma unroll
      for (int d = 0; d < DH; ++d) s += q[d] * ks[j][d];
      if (s > m) { float f = exp2f(m - s); l *= f;
#pragma unroll
        for (int d = 0; d < DH; ++d) acc[d] *= f;
        m = s; }
      float p = b2f(f2b(exp2f(s - m))); l += p;
#pragma unroll
      for (int d = 0; d < DH; ++d) acc[d] += p * vs[j][d]; } }
#pragma unroll
  for (int d = 0; d < DH; ++d) o[(size_t)t * D_ + hh * DH + d] = b2f(f2b(acc[d] / l));
}
__global__ void k_out(const float* pr, const float* o, const float* h, const u16* x, const u16* wo, const u16* cre, const u16* cim, const u16* dsk, float* out) {
  __shared__ float os[8][D_], hs[8][896];
  int t0 = blockIdx.x * 8;
  for (int i = threadIdx.x; i < 8 * D_; i += 448) os[i / D_][i % D_] = o[(size_t)t0 * D_ + i];
  for (int i = threadIdx.x; i < 8 * 896; i += 448) hs[i / 896][i % 896] = h[(size_t)t0 * 896 + i];
  __syncthreads();
  int d = threadIdx.x; float a[8], y[8];
#pragma unroll
  for (int i = 0; i < 8; ++i) { a[i] = 0.f; y[i] = 0.f; }
  for (int k = 0; k < D_; ++k) { float w = b2f(wo[(size_t)k * D_ + d]), cr = b2f(cre[(size_t)d * D_ + k]), ci = b2f(cim[(size_t)d * D_ + k]);
#pragma unroll
    for (int i = 0; i < 8; ++i) { a[i] += os[i][k] * w; y[i] += hs[i][k] * cr - hs[i][448 + k] * ci; } }
#pragma unroll
  for (int i = 0; i < 8; ++i) { int t = t0 + i; float g = 1.f / (1.f + expf(-pr[(size_t)t * NP + 1400]));
    float b = 2.f * y[i] + b2f(x[(size_t)t * D_ + d]) * b2f(dsk[d]); out[(size_t)t * D_ + d] = g * a[i] + (1.f - g) * b; }
}
extern "C" void kernel_launch(void* const* d_in, const int* in_sizes, int n_in, void* d_out, int out_size, void* d_ws, size_t ws_size, hipStream_t stream) {
  const u16 *x = (const u16*)d_in[0], *wq = (const u16*)d_in[1], *wk = (const u16*)d_in[2], *wv = (const u16*)d_in[3], *wo = (const u16*)d_in[4];
  const u16 *lre = (const u16*)d_in[6], *lim = (const u16*)d_in[7], *bre = (const u16*)d_in[8], *bim = (const u16*)d_in[9], *cre = (const u16*)d_in[10], *cim = (const u16*)d_in[11], *ldt = (const u16*)d_in[12], *dsk = (const u16*)d_in[13], *gw = (const u16*)d_in[14];
  float* pr = (float*)d_ws; float* h = pr + (size_t)S_ * NP; float* o = h + (size_t)S_ * 896;
  k_proj<<<dim3(S_ / 16, 6), 256, 0, stream>>>(x, wq, wk, wv, bre, bim, gw, pr);
  k_scan<<<7, 64, 0, stream>>>(pr, lre, lim, ldt, h);
  k_attn<<<S_ * H_ / 64, 64, 0, stream>>>(pr, o);
  k_out<<<S_ / 8, 448, 0, stream>>>(pr, o, h, x, wo, cre, cim, dsk, (float*)d_out);
}
```

```cpp
#include <hip/hip_runtime.h>
#include <stdint.h>

struct Srd { const void* p; uint32_t bytes; uint32_t fmt; };
struct Args { Srd b[16]; };

__global__ void __launch_bounds__(256, 1) gated_mix_fused_kernel(Args args) {
  __shared__ __attribute__((aligned(16))) char lds[163840];
  const void* ka = (const void*)__builtin_amdgcn_kernarg_segment_ptr();
  unsigned bid = blockIdx.x, tid = threadIdx.x;
  unsigned ldsbase = (unsigned)(uintptr_t)(__attribute__((address_space(3))) char*)lds;
  asm volatile(
    "s_load_dwordx16 s[8:23], %0, 0x0\n"
    "s_load_dwordx16 s[24:39], %0, 0x40\n"
    "s_load_dwordx16 s[40:55], %0, 0x80\n"
    "s_load_dwordx16 s[56:71], %0, 0xc0\n"
    "s_getpc_b64 s[82:83]\n"
    "Lpc_%=:\n"
    "v_lshrrev_b32 v6, 6, %2\n"
    "v_and_b32 v7, 1, %2\n"
    "s_and_b32 s84, %1, 248\n"
    "v_mad_u32_u24 v6, v6, 2, v7\n"
    "s_mul_i32 s84, s84, 128\n"
    "v_lshl_add_u32 v6, v6, 7, s84\n"
    "v_min_u32 v14, Lend_%=-Lpc_%=-4, v6\n"
    "global_load_dword v13, v14, s[82:83]\n"
    "v_lshrrev_b32 v5, 6, %2\n"
    "v_and_b32 v1, 63, %2\n"
    "v_readfirstlane_b32 s72, v5\n"
    "s_bfe_u32 s84, %1, 0x20001\n"
    "s_bfe_u32 s77, %1, 0x30003\n"
    "s_lshl3_add_u32 s76, s84, s77\n"
    "s_and_b32 s84, %1, 1\n"
    "s_lshr_b32 s87, %1, 6\n"
    "s_lshl2_add_u32 s75, s84, s87\n"
    "s_and_b32 s73, s72, 1\n"
    "s_lshr_b32 s74, s72, 1\n"
    "s_lshl2_add_u32 s84, %1, s72\n"
    "s_mul_i32 s85, s84, 52429\n"
    "s_lshr_b32 s85, s85, 18\n"
    "s_mul_i32 s86, s85, 5\n"
    "s_sub_u32 s84, s84, s86\n"
    "s_lshl4_add_u32 s84, s84, 64\n"
    "s_load_dwordx4 s[88:91], %0, s84\n"
    "v_min_u32 v11, 15, v1\n"
    "v_lshlrev_b32 v11, 7, v11\n"
    "s_mul_i32 s85, s85, 2048\n"
    "v_add_u32 v11, s85, v11\n"
    "v_lshrrev_b32 v16, 3, v1\n"
    "v_and_b32 v17, 7, v1\n"
    "v_and_b32 v2, 31, v1\n"
    "v_lshrrev_b32 v3, 5, v1\n"
    "v_lshrrev_b32 v5, 1, v16\n"
    "v_xor_b32 v18, v17, v5\n"
    "v_bfe_u32 v7, v1, 3, 1\n"
    "v_bfe_u32 v23, v1, 4, 1\n"
    "v_lshl_or_b32 v6, v3, 4, v23\n"
    "v_lshl_or_b32 v6, v7, 3, v6\n"
    "s_lshl3_add_u32 s84, s74, s73\n"
    "s_lshl_b32 s84, s84, 2\n"
    "v_add_u32 v6, s84, v6\n"
    "v_mul_u32_u24 v6, 896, v6\n"
    "v_lshl_add_u32 v19, v18, 4, v6\n"
    "v_xor_b32 v7, 4, v18\n"
    "v_add_u32 v6, 768, v6\n"
    "v_lshl_add_u32 v20, v7, 4, v6\n"
    "s_mul_i32 s92, s76, 57344\n"
    "s_lshl_b32 s93, s72, 11\n"
    "s_lshl1_add_u32 s94, s87, s77\n"
    "s_sub_u32 s84, s94, 7\n"
    "s_cselect_b32 s94, s94, s84\n"
    "s_lshl_b32 s94, s94, 7\n"
    "v_lshlrev_b32 v8, 2, v23\n"
    "v_xor_b32 v8, v17, v8\n"
    "s_lshl_b32 s84, s72, 4\n"
    "v_add_u32 v9, s84, v16\n"
    "v_mul_u32_u24 v9, 896, v9\n"
    "s_lshl_b32 s85, s75, 7\n"
    "v_lshl_add_u32 v8, v8, 4, s85\n"
    "v_add_u32 v8, v8, v9\n"
    "v_add_u32 v9, 7168, v8\n"
    "v_and_b32 v22, 3, v1\n"
    "v_lshrrev_b32 v10, 2, v1\n"
    "v_add_u32 v10, s84, v10\n"
    "v_mul_u32_u24 v10, 56, v10\n"
    "v_lshlrev_b32 v5, 4, v22\n"
    "v_min_u32 v5, 40, v5\n"
    "v_add_u32 v10, v10, v5\n"
    "s_cmp_eq_u32 s75, 7\n"
    "s_cselect_b64 s[80:81], -1, 0\n"
    "s_cselect_b32 s96, 28, 448\n"
    "v_cndmask_b32 v24, v8, v10, s[80:81]\n"
    "v_cndmask_b32 v25, v9, v10, s[80:81]\n"
    "s_waitcnt lgkmcnt(0)\n"
    "s_load_dwordx2 s[78:79], s[48:49], 0x0\n"
    "s_cselect_b64 s[20:21], s[20:21], s[12:13]\n"
    "s_cselect_b32 s22, s22, s14\n"
    "s_cselect_b64 s[12:13], s[16:17], s[12:13]\n"
    "s_cselect_b32 s14, s18, s14\n"
    "s_sub_u32 s20, s20, 1024\n"
    "s_subb_u32 s21, s21, 0\n"
    "s_add_u32 s22, s22, 1024\n"
    "v_mov_b32 v26, v11\n"
    "s_add_u32 m0, s93, 0\n"
    "s_add_u32 s95, s94, s92\n"
    "buffer_load_dwordx4 v19, s[8:11], s95 offen  lds\n"
    "s_mul_i32 s97, s94, s96\n"
    "buffer_load_dwordx4 v20, s[8:11], s95 offen offset:1024  lds\n"
    "s_add_u32 m0, s93, 106496\n"
    "s_add_u32 s94, s94, 128\n"
    "s_cmp_eq_u32 s94, 896\n"
    "s_cselect_b32 s94, 0, s94\n"
    "buffer_load_dwordx4 v24, s[12:15], s97 offen  lds\n"
    "buffer_load_dwordx4 v25, s[20:23], s97 offen offset:1024  lds\n"
    "s_add_u32 m0, s93, 8192\n"
    "s_add_u32 s95, s94, s92\n"
    "buffer_load_dwordx4 v19, s[8:11], s95 offen  lds\n"
    "s_mul_i32 s97, s94, s96\n"
    "buffer_load_dwordx4 v20, s[8:11], s95 offen offset:1024  lds\n"
    "s_add_u32 m0, s93, 114688\n"
    "s_add_u32 s94, s94, 128\n"
    "s_cmp_eq_u32 s94, 896\n"
    "s_cselect_b32 s94, 0, s94\n"
    "buffer_load_dwordx4 v24, s[12:15], s97 offen  lds\n"
    "buffer_load_dwordx4 v25, s[20:23], s97 offen offset:1024  lds\n"
    "s_mul_i32 s77, s75, 112\n"
    "s_lshl_b32 s63, s74, 6\n"
    "s_add_u32 s77, s77, s63\n"
    "s_mov_b32 s63, s59\n"
    "v_lshlrev_b32 v29, 1, v2\n"
    "buffer_load_ushort v27, v29, s[56:59], s77 offen\n"
    "buffer_load_ushort v28, v29, s[60:63], s77 offen\n"
    "buffer_load_ushort v29, v29, s[64:67], s77 offen\n"
    "s_and_b32 s77, s73, 1\n"
    "s_cselect_b64 s[56:57], s[68:69], s[44:45]\n"
    "v_lshlrev_b32 v34, 7, v17\n"
    "s_lshr_b32 s77, %1, 3\n"
    "s_or_b32 s77, s77, s74\n"
    "s_lshl_b32 s77, s77, 12\n"
    "buffer_load_dword v13, v34, s[56:59], s77 offen\n"
    "s_add_u32 m0, s93, 16384\n"
    "s_add_u32 s95, s94, s92\n"
    "buffer_load_dwordx4 v19, s[8:11], s95 offen  lds\n"
    "s_mul_i32 s97, s94, s96\n"
    "buffer_load_dwordx4 v20, s[8:11], s95 offen offset:1024  lds\n"
    "s_add_u32 m0, s93, 122880\n"
    "s_add_u32 s94, s94, 128\n"
    "s_cmp_eq_u32 s94, 896\n"
    "s_cselect_b32 s94, 0, s94\n"
    "buffer_load_dwordx4 v24, s[12:15], s97 offen  lds\n"
    "v_lshlrev_b32 v4, 4, v1\n"
    "v_bfe_u32 v45, v1, 1, 3\n"
    "v_xor_b32 v5, v45, v3\n"
    "v_lshlrev_b32 v5, 4, v5\n"
    "v_lshl_or_b32 v5, v2, 7, v5\n"
    "s_lshl_b32 s84, s73, 12\n"
    "s_lshl_b32 s85, s74, 12\n"
    "s_add_u32 s85, s85, 57344\n"
    "v_add_u32 v35, s84, v5\n"
    "v_add_u32 v39, s85, v5\n"
    "v_xor_b32 v6, 32, v5\n"
    "buffer_load_dwordx4 v25, s[20:23], s97 offen offset:1024  lds\n"
    "v_add_u32 v36, s84, v6\n"
    "v_add_u32 v40, s85, v6\n"
    "v_xor_b32 v6, 64, v5\n"
    "v_add_u32 v37, s84, v6\n"
    "v_add_u32 v41, s85, v6\n"
    "v_xor_b32 v6, 96, v5\n"
    "v_add_u32 v38, s84, v6\n"
    "v_add_u32 v42, s85, v6\n"
    "v_bfe_u32 v21, v1, 2, 2\n"
    "v_and_b32 v5, 1, v22\n"
    "v_lshrrev_b32 v6, 1, v22\n"
    "s_add_u32 m0, s93, 24576\n"
    "s_add_u32 s95, s94, s92\n"
    "buffer_load_dwordx4 v19, s[8:11], s95 offen  lds\n"
    "v_lshl_or_b32 v5, v5, 1, v6\n"
    "v_lshl_or_b32 v5, v23, 2, v5\n"
    "v_cmp_lt_u32 vcc, 5, v5\n"
    "v_lshl_or_b32 v6, v23, 2, v22\n"
    "v_lshrrev_b32 v7, 1, v21\n"
    "v_cndmask_b32 v5, v5, 7, vcc\n"
    "v_xor_b32 v7, s74, v7\n"
    "v_lshl_or_b32 v6, v7, 3, v6\n"
    "v_lshl_add_u32 v8, v3, 3, v21\n"
    "s_lshl_b32 s84, s74, 7\n"
    "v_lshl_add_u32 v6, v8, 4, v6\n"
    "s_mul_i32 s97, s94, s96\n"
    "buffer_load_dwordx4 v20, s[8:11], s95 offen offset:1024  lds\n"
    "v_lshl_add_u32 v5, v8, 3, v5\n"
    "v_add_u32 v5, s84, v5\n"
    "v_cndmask_b32 v5, v6, v5, s[80:81]\n"
    "v_lshlrev_b32 v5, 3, v5\n"
    "s_cmp_eq_u32 s75, 7\n"
    "s_cselect_b32 s84, 1, 2\n"
    "s_lshl_b32 s84, s84, 8\n"
    "v_add_u32 v43, 106496, v5\n"
    "v_add_u32 v44, s84, v43\n"
    "v_mul_u32_u24 v5, 896, v16\n"
    "v_xor_b32 v6, 4, v18\n"
    "s_add_u32 m0, s93, 131072\n"
    "s_add_u32 s94, s94, 128\n"
    "s_cmp_eq_u32 s94, 896\n"
    "s_cselect_b32 s94, 0, s94\n"
    "buffer_load_dwordx4 v24, s[12:15], s97 offen  lds\n"
    "v_lshl_add_u32 v46, v18, 4, v5\n"
    "v_lshl_add_u32 v47, v6, 4, v5\n"
    "s_cmp_eq_u32 s73, 1\n"
    "s_cselect_b64 vcc, -1, 0\n"
    "s_cselect_b32 s86, 2, 3\n"
    "v_cndmask_b32 v48, v46, v47, vcc\n"
    "s_cmp_eq_u32 s74, 0\n"
    "s_cselect_b64 s[24:25], s[24:25], s[28:29]\n"
    "s_cselect_b64 s[32:33], s[32:33], s[36:37]\n"
    "s_mul_i32 s30, s75, 7\n"
    "s_add_u32 s30, s30, s73\n"
    "buffer_load_dwordx4 v25, s[20:23], s97 offen offset:1024  lds\n"
    "s_mul_i32 s30, s30, 7168\n"
    "s_sub_u32 s44, s44, s30\n"
    "s_subb_u32 s45, s45, 0\n"
    "s_add_u32 s46, s46, s30\n"
    "s_cmp_eq_u32 s72, 1\n"
    "s_cselect_b64 s[16:17], s[44:45], s[24:25]\n"
    "s_cselect_b64 s[18:19], s[46:47], s[26:27]\n"
    "s_cselect_b64 s[82:83], 0xff, -1\n"
    "s_cselect_b32 s86, 0, s86\n"
    "s_mul_i32 s86, s86, 14336\n"
    "v_add_u32 v49, 12288, v48\n"
    "s_add_u32 m0, s93, 32768\n"
    "s_add_u32 s95, s94, s92\n"
    "buffer_load_dwordx4 v19, s[8:11], s95 offen  lds\n"
    "v_add_u32 v50, 28672, v48\n"
    "v_add_u32 v51, s86, v48\n"
    "s_mul_i32 s84, s74, 7296\n"
    "s_lshl_b32 s85, s73, 10\n"
    "s_add_u32 s31, s84, s85\n"
    "s_add_u32 s38, s31, 6144\n"
    "s_add_u32 s84, s31, 4096\n"
    "s_cmp_eq_u32 s72, 3\n"
    "s_cselect_b32 s38, s84, s38\n"
    "s_mul_i32 s28, s75, 112\n"
    "s_lshl_b32 s85, s74, 6\n"
    "s_mul_i32 s97, s94, s96\n"
    "buffer_load_dwordx4 v20, s[8:11], s95 offen offset:1024  lds\n"
    "s_add_u32 s28, s28, s85\n"
    "v_cmp_gt_u32 vcc, 8, v1\n"
    "v_mov_b32 v5, 2147483648\n"
    "v_mov_b32 v53, s73\n"
    "v_cndmask_b32 v52, v5, v4, vcc\n"
    "v_mov_b32 v5, 1049335798\n"
    "v_lshlrev_b32 v97, 3, v2\n"
    "v_cndmask_b32 v90, v5, 1.0, s[80:81]\n"
    "v_lshl_add_u32 v84, v3, 8, v97\n"
    "s_lshl_b32 s84, s73, 11\n"
    "s_add_u32 s84, s84, 100736\n"
    "s_add_u32 m0, s93, 139264\n"
    "s_add_u32 s94, s94, 128\n"
    "s_cmp_eq_u32 s94, 896\n"
    "s_cselect_b32 s94, 0, s94\n"
    "buffer_load_dwordx4 v24, s[12:15], s97 offen  lds\n"
    "v_lshl_add_u32 v6, v2, 6, s84\n"
    "v_bfe_u32 v7, v2, 1, 2\n"
    "v_xor_b32 v7, v7, v3\n"
    "v_lshl_add_u32 v85, v7, 4, v6\n"
    "v_lshl_add_u32 v6, v3, 2, v21\n"
    "v_lshrrev_b32 v7, 1, v6\n"
    "v_lshl_add_u32 v6, v6, 6, s84\n"
    "v_lshl_or_b32 v8, v23, 2, v22\n"
    "v_lshlrev_b32 v7, 1, v7\n"
    "v_xor_b32 v8, v8, v7\n"
    "v_lshl_add_u32 v86, v8, 3, v6\n"
    "buffer_load_dwordx4 v25, s[20:23], s97 offen offset:1024  lds\n"
    "buffer_load_dword v13, v26, s[88:91], 0 offen\n"
    "v_cmp_gt_u32 vcc, 28, v2\n"
    "v_mov_b32 v5, 0x3f803f80\n"
    "s_nop 0\n"
    "v_cndmask_b32 v87, 0, -1, vcc\n"
    "v_cmp_eq_u32 vcc, 28, v2\n"
    "s_nop 1\n"
    "v_cndmask_b32 v88, 0, v5, vcc\n"
    "s_lshl1_add_u32 s64, s76, s73\n"
    "s_lshl1_add_u32 s65, s75, s74\n"
    "s_mul_i32 s60, s64, 14\n"
    "s_add_u32 s60, s60, s65\n"
    "s_lshl_b32 s60, s60, 11\n"
    "s_add_u32 s60, s60, 6291456\n"
    "s_lshl1_add_u32 s85, s64, s74\n"
    "s_lshl_b32 s85, s85, 11\n"
    "s_add_u32 s85, s85, 262144\n"
    "s_cmp_eq_u32 s75, 7\n"
    "s_cselect_b32 s60, s85, s60\n"
    "s_lshl3_add_u32 s61, s76, s75\n"
    "s_lshl_b32 s61, s61, 7\n"
    "s_add_u32 s61, s61, 8912896\n"
    "s_lshl_b32 s84, s72, 6\n"
    "s_add_u32 s84, s84, 7\n"
    "v_lshl_add_u32 v5, v17, 3, s84\n"
    "v_and_b32 v6, 1, v1\n"
    "s_mul_i32 s84, s75, 7\n"
    "s_lshr_b32 s84, s84, 3\n"
    "s_lshl3_add_u32 s84, s76, s84\n"
    "v_cmp_eq_u32 vcc, 0, v3\n"
    "v_add_u32 v6, s84, v6\n"
    "v_lshl_add_u32 v82, v3, 9, v97\n"
    "v_cndmask_b32 v5, v6, v5, vcc\n"
    "v_lshl_or_b32 v83, v3, 31, v82\n"
    "v_lshlrev_b32 v58, 7, v5\n"
    "s_cmp_eq_u32 s72, 0\n"
    "s_cselect_b32 s84, 0, 2147483648\n"
    "v_lshlrev_b32 v5, 4, v17\n"
    "v_lshl_or_b32 v5, v16, 15, v5\n"
    "v_or_b32 v98, s84, v5\n"
    "s_and_b32 s84, %1, 7\n"
    "s_lshl_b32 s84, s84, 15\n"
    "v_add_u32 v58, s84, v58\n"
    "s_lshl_b32 s85, s72, 16\n"
    "s_add_u32 s85, s85, 266240\n"
    "s_add_u32 s36, s48, s85\n"
    "s_addc_u32 s37, s49, 0\n"
    "s_lshl4_add_u32 s84, s76, s75\n"
    "s_mul_i32 s84, s84, 3584\n"
    "s_add_u32 s84, s84, 6291456\n"
    "s_lshl_b32 s85, s72, 3\n"
    "s_mov_b32 s80, 0x11011000\n"
    "s_mov_b32 s81, 0x11011000\n"
    "s_waitcnt vmcnt(21)\n"
    "s_waitcnt lgkmcnt(0)\n"
    "s_barrier\n"
    "ds_read_b128 a[0:3], v35 offset:0\n"
    "ds_read_b64_tr_b16 v[208:209], v43 offset:0\n"
    "ds_read_b64_tr_b16 v[210:211], v44 offset:0\n"
    "ds_read_b128 a[4:7], v36 offset:0\n"
    "ds_read_b64_tr_b16 v[212:213], v43 offset:2048\n"
    "ds_read_b64_tr_b16 v[214:215], v44 offset:2048\n"
    "ds_read_b128 a[8:11], v37 offset:0\n"
    "ds_read_b64_tr_b16 v[216:217], v43 offset:4096\n"
    "ds_read_b64_tr_b16 v[218:219], v44 offset:4096\n"
    "ds_read_b128 a[12:15], v38 offset:0\n"
    "ds_read_b64_tr_b16 v[220:221], v43 offset:6144\n"
    "ds_read_b64_tr_b16 v[222:223], v44 offset:6144\n"
    "s_waitcnt vmcnt(17)\n"
    "s_barrier\n"
    "s_add_u32 m0, s93, 40960\n"
    "s_add_u32 s95, s94, s92\n"
    "s_waitcnt lgkmcnt(9)\n"
    "v_mfma_f32_32x32x16_bf16 v[160:175], v[208:211], a[0:3], 0\n"
    "ds_read_b128 a[16:19], v35 offset:8192\n"
    "buffer_load_dwordx4 v19, s[8:11], s95 offen  lds\n"
    "ds_read_b64_tr_b16 v[176:177], v43 offset:8192\n"
    "ds_read_b64_tr_b16 v[178:179], v44 offset:8192\n"
    "s_lshr_b64 s[80:81], s[80:81], s85\n"
    "s_mul_i32 s97, s94, s96\n"
    "s_waitcnt lgkmcnt(9)\n"
    "v_mfma_f32_32x32x16_bf16 v[160:175], v[212:215], a[4:7], v[160:175]\n"
    "ds_read_b128 a[20:23], v36 offset:8192\n"
    "buffer_load_dwordx4 v20, s[8:11], s95 offen offset:1024  lds\n"
    "ds_read_b64_tr_b16 v[180:181], v43 offset:10240\n"
    "ds_read_b64_tr_b16 v[182:183], v44 offset:10240\n"
    "s_bfe_u32 s85, s80, 524288\n"
    "s_add_u32 m0, s93, 147456\n"
    "s_add_u32 s94, s94, 128\n"
    "s_cmp_eq_u32 s94, 896\n"
    "s_cselect_b32 s94, 0, s94\n"
    "s_waitcnt lgkmcnt(9)\n"
    "v_mfma_f32_32x32x16_bf16 v[160:175], v[216:219], a[8:11], v[160:175]\n"
    "ds_read_b128 a[24:27], v37 offset:8192\n"
    "buffer_load_dwordx4 v24, s[12:15], s97 offen  lds\n"
    "ds_read_b64_tr_b16 v[184:185], v43 offset:12288\n"
    "ds_read_b64_tr_b16 v[186:187], v44 offset:12288\n"
    "s_mul_i32 s85, s85, 1792\n"
    "s_waitcnt lgkmcnt(9)\n"
    "v_mfma_f32_32x32x16_bf16 v[160:175], v[220:223], a[12:15], v[160:175]\n"
    "ds_read_b128 a[28:31], v38 offset:8192\n"
    "buffer_load_dwordx4 v25, s[20:23], s97 offen offset:1024  lds\n"
    "ds_read_b64_tr_b16 v[188:189], v43 offset:14336\n"
    "ds_read_b64_tr_b16 v[190:191], v44 offset:14336\n"
    "s_add_u32 s66, s85, s84\n"
    "s_waitcnt vmcnt(13)\n"
    "s_barrier\n"
    "s_add_u32 m0, s93, 49152\n"
    "s_add_u32 s95, s94, s92\n"
    "s_waitcnt lgkmcnt(9)\n"
    "v_mfma_f32_32x32x16_bf16 v[160:175], v[176:179], a[16:19], v[160:175]\n"
    "ds_read_b128 a[32:35], v35 offset:16384\n"
    "buffer_load_dwordx4 v19, s[8:11], s95 offen  lds\n"
    "ds_read_b64_tr_b16 v[208:209], v43 offset:16384\n"
    "ds_read_b64_tr_b16 v[210:211], v44 offset:16384\n"
    "s_bfe_u32 s85, s80, 524296\n"
    "s_mul_i32 s97, s94, s96\n"
    "s_waitcnt lgkmcnt(9)\n"
    "v_mfma_f32_32x32x16_bf16 v[160:175], v[180:183], a[20:23], v[160:175]\n"
    "ds_read_b128 a[36:39], v36 offset:16384\n"
    "buffer_load_dwordx4 v20, s[8:11], s95 offen offset:1024  lds\n"
    "ds_read_b64_tr_b16 v[212:213], v43 offset:18432\n"
    "ds_read_b64_tr_b16 v[214:215], v44 offset:18432\n"
    "s_mul_i32 s85, s85, 1792\n"
    "s_add_u32 m0, s93, 155648\n"
    "s_add_u32 s94, s94, 128\n"
    "s_cmp_eq_u32 s94, 896\n"
    "s_cselect_b32 s94, 0, s94\n"
    "s_waitcnt lgkmcnt(9)\n"
    "v_mfma_f32_32x32x16_bf16 v[160:175], v[184:187], a[24:27], v[160:175]\n"
    "ds_read_b128 a[40:43], v37 offset:16384\n"
    "buffer_load_dwordx4 v24, s[12:15], s97 offen  lds\n"
    "ds_read_b64_tr_b16 v[216:217], v43 offset:20480\n"
    "ds_read_b64_tr_b16 v[218:219], v44 offset:20480\n"
    "s_add_u32 s67, s85, s84\n"
    "s_waitcnt lgkmcnt(9)\n"
    "v_mfma_f32_32x32x16_bf16 v[160:175], v[188:191], a[28:31], v[160:175]\n"
    "ds_read_b128 a[44:47], v38 offset:16384\n"
    "buffer_load_dwordx4 v25, s[20:23], s97 offen offset:1024  lds\n"
    "ds_read_b64_tr_b16 v[220:221], v43 offset:22528\n"
    "ds_read_b64_tr_b16 v[222:223], v44 offset:22528\n"
    "s_bfe_u32 s85, s80, 524304\n"
    "s_waitcnt vmcnt(13)\n"
    "s_barrier\n"
    "s_add_u32 m0, s31, 57344\n"
    "s_add_u32 s95, s94, s30\n"
    "s_waitcnt lgkmcnt(9)\n"
    "v_mfma_f32_32x32x16_bf16 v[160:175], v[208:211], a[32:35], v[160:175]\n"
    "ds_read_b128 a[48:51], v35 offset:24576\n"
    "buffer_load_dwordx4 v48, s[24:27], s95 offen offset:0 lds\n"
    "ds_read_b64_tr_b16 v[176:177], v43 offset:24576\n"
    "ds_read_b64_tr_b16 v[178:179], v44 offset:24576\n"
    "s_mul_i32 s85, s85, 1792\n"
    "s_add_u32 s94, s94, 128\n"
    "s_cmp_eq_u32 s94, 896\n"
    "s_cselect_b32 s94, 0, s94\n"
    "s_waitcnt lgkmcnt(9)\n"
    "v_mfma_f32_32x32x16_bf16 v[160:175], v[212:215], a[36:39], v[160:175]\n"
    "ds_read_b128 a[52:55], v36 offset:24576\n"
    "buffer_load_dwordx4 v49, s[24:27], s95 offen offset:2048 lds\n"
    "s_add_u32 m0, s31, 61440\n"
    "ds_read_b64_tr_b16 v[180:181], v43 offset:26624\n"
    "ds_read_b64_tr_b16 v[182:183], v44 offset:26624\n"
    "s_add_u32 s77, s85, s84\n"
    "s_waitcnt lgkmcnt(9)\n"
    "v_mfma_f32_32x32x16_bf16 v[160:175], v[216:219], a[40:43], v[160:175]\n"
    "ds_read_b128 a[56:59], v37 offset:24576\n"
    "buffer_load_dwordx4 v50, s[24:27], s95 offen offset:0 lds\n"
    "s_add_u32 m0, s38, 57344\n"
    "ds_read_b64_tr_b16 v[184:185], v43 offset:28672\n"
    "ds_read_b64_tr_b16 v[186:187], v44 offset:28672\n"
    "s_bfe_u32 s85, s80, 524312\n"
    "s_waitcnt lgkmcnt(9)\n"
    "v_mfma_f32_32x32x16_bf16 v[160:175], v[220:223], a[44:47], v[160:175]\n"
    "ds_read_b128 a[60:63], v38 offset:24576\n"
    "s_mov_b64 exec, s[82:83]\n"
    "buffer_load_dwordx4 v51, s[16:19], s95 offen offset:0 lds\n"
    "s_mov_b64 exec, -1\n"
    "ds_read_b64_tr_b16 v[188:189], v43 offset:30720\n"
    "ds_read_b64_tr_b16 v[190:191], v44 offset:30720\n"
    "s_mul_i32 s85, s85, 1792\n"
    "s_waitcnt vmcnt(13)\n"
    "s_barrier\n"
    "s_add_u32 m0, s31, 71808\n"
    "s_add_u32 s95, s94, s30\n"
    "s_waitcnt lgkmcnt(9)\n"
    "v_mfma_f32_32x32x16_bf16 v[160:175], v[176:179], a[48:51], v[160:175]\n"
    "ds_read_b128 a[64:67], v35 offset:32768\n"
    "buffer_load_dwordx4 v48, s[24:27], s95 offen offset:0 lds\n"
    "ds_read_b64_tr_b16 v[208:209], v43 offset:32768\n"
    "ds_read_b64_tr_b16 v[210:211], v44 offset:32768\n"
    "s_add_u32 s88, s85, s84\n"
    "s_add_u32 s94, s94, 128\n"
    "s_cmp_eq_u32 s94, 896\n"
    "s_cselect_b32 s94, 0, s94\n"
    "s_waitcnt lgkmcnt(9)\n"
    "v_mfma_f32_32x32x16_bf16 v[160:175], v[180:183], a[52:55], v[160:175]\n"
    "ds_read_b128 a[68:71], v36 offset:32768\n"
    "buffer_load_dwordx4 v49, s[24:27], s95 offen offset:2048 lds\n"
    "s_add_u32 m0, s31, 75904\n"
    "ds_read_b64_tr_b16 v[212:213], v43 offset:34816\n"
    "ds_read_b64_tr_b16 v[214:215], v44 offset:34816\n"
    "s_waitcnt lgkmcnt(9)\n"
    "v_mfma_f32_32x32x16_bf16 v[160:175], v[184:187], a[56:59], v[160:175]\n"
    "ds_read_b128 a[72:75], v37 offset:32768\n"
    "buffer_load_dwordx4 v50, s[24:27], s95 offen offset:0 lds\n"
    "s_add_u32 m0, s38, 71808\n"
    "ds_read_b64_tr_b16 v[216:217], v43 offset:36864\n"
    "ds_read_b64_tr_b16 v[218:219], v44 offset:36864\n"
    "s_waitcnt lgkmcnt(9)\n"
    "v_mfma_f32_32x32x16_bf16 v[160:175], v[188:191], a[60:63], v[160:175]\n"
    "ds_read_b128 a[76:79], v38 offset:32768\n"
    "s_mov_b64 exec, s[82:83]\n"
    "buffer_load_dwordx4 v51, s[16:19], s95 offen offset:0 lds\n"
    "s_mov_b64 exec, -1\n"
    "ds_read_b64_tr_b16 v[220:221], v43 offset:38912\n"
    "ds_read_b64_tr_b16 v[222:223], v44 offset:38912\n"
    "s_waitcnt vmcnt(12)\n"
    "s_barrier\n"
    "s_add_u32 m0, s31, 86272\n"
    "s_add_u32 s95, s94, s30\n"
    "s_waitcnt lgkmcnt(9)\n"
    "v_mfma_f32_32x32x16_bf16 v[160:175], v[208:211], a[64:67], v[160:175]\n"
    "ds_read_b128 a[80:83], v35 offset:40960\n"
    "buffer_load_dwordx4 v48, s[24:27], s95 offen offset:0 lds\n"
    "ds_read_b64_tr_b16 v[176:177], v43 offset:40960\n"
    "ds_read_b64_tr_b16 v[178:179], v44 offset:40960\n"
    "s_add_u32 s94, s94, 128\n"
    "s_cmp_eq_u32 s94, 896\n"
    "s_cselect_b32 s94, 0, s94\n"
    "s_waitcnt lgkmcnt(9)\n"
    "v_mfma_f32_32x32x16_bf16 v[160:175], v[212:215], a[68:71], v[160:175]\n"
    "ds_read_b128 a[84:87], v36 offset:40960\n"
    "buffer_load_dwordx4 v49, s[24:27], s95 offen offset:2048 lds\n"
    "s_add_u32 m0, s31, 90368\n"
    "ds_read_b64_tr_b16 v[180:181], v43 offset:43008\n"
    "ds_read_b64_tr_b16 v[182:183], v44 offset:43008\n"
    "s_waitcnt lgkmcnt(9)\n"
    "v_mfma_f32_32x32x16_bf16 v[160:175], v[216:219], a[72:75], v[160:175]\n"
    "ds_read_b128 a[88:91], v37 offset:40960\n"
    "buffer_load_dwordx4 v50, s[24:27], s95 offen offset:0 lds\n"
    "s_add_u32 m0, s38, 86272\n"
    "ds_read_b64_tr_b16 v[184:185], v43 offset:45056\n"
    "ds_read_b64_tr_b16 v[186:187], v44 offset:45056\n"
    "s_waitcnt lgkmcnt(9)\n"
    "v_mfma_f32_32x32x16_bf16 v[160:175], v[220:223], a[76:79], v[160:175]\n"
    "ds_read_b128 a[92:95], v38 offset:40960\n"
    "s_mov_b64 exec, s[82:83]\n"
    "buffer_load_dwordx4 v51, s[16:19], s95 offen offset:0 lds\n"
    "s_mov_b64 exec, -1\n"
    "ds_read_b64_tr_b16 v[188:189], v43 offset:47104\n"
    "ds_read_b64_tr_b16 v[190:191], v44 offset:47104\n"
    "s_waitcnt vmcnt(12)\n"
    "s_barrier\n"
    "s_waitcnt lgkmcnt(9)\n"
    "v_mfma_f32_32x32x16_bf16 v[160:175], v[176:179], a[80:83], v[160:175]\n"
    "ds_read_b128 a[96:99], v35 offset:49152\n"
    "ds_read_b64_tr_b16 v[208:209], v43 offset:49152\n"
    "ds_read_b64_tr_b16 v[210:211], v44 offset:49152\n"
    "s_waitcnt lgkmcnt(9)\n"
    "v_mfma_f32_32x32x16_bf16 v[160:175], v[180:183], a[84:87], v[160:175]\n"
    "ds_read_b128 a[100:103], v36 offset:49152\n"
    "ds_read_b64_tr_b16 v[212:213], v43 offset:51200\n"
    "ds_read_b64_tr_b16 v[214:215], v44 offset:51200\n"
    "s_waitcnt lgkmcnt(9)\n"
    "v_mfma_f32_32x32x16_bf16 v[160:175], v[184:187], a[88:91], v[160:175]\n"
    "ds_read_b128 a[104:107], v37 offset:49152\n"
    "ds_read_b64_tr_b16 v[216:217], v43 offset:53248\n"
    "ds_read_b64_tr_b16 v[218:219], v44 offset:53248\n"
    "s_waitcnt lgkmcnt(9)\n"
    "v_mfma_f32_32x32x16_bf16 v[160:175], v[188:191], a[92:95], v[160:175]\n"
    "ds_read_b128 a[108:111], v38 offset:49152\n"
    "ds_read_b64_tr_b16 v[220:221], v43 offset:55296\n"
    "ds_read_b64_tr_b16 v[222:223], v44 offset:55296\n"
    "s_waitcnt lgkmcnt(0)\n"
    "s_barrier\n"
    "s_add_u32 m0, s31, 105984\n"
    "s_add_u32 s95, s94, s30\n"
    "v_mfma_f32_32x32x16_bf16 v[160:175], v[208:211], a[96:99], v[160:175]\n"
    "buffer_load_dwordx4 v48, s[24:27], s95 offen offset:0 lds\n"
    "s_mul_i32 s84, s79, 1284865837\n"
    "s_mul_i32 s85, s78, 1481765933\n"
    "s_mul_hi_u32 s86, s78, 1284865837\n"
    "s_mul_i32 s78, s78, 1284865837\n"
    "s_add_u32 s84, s84, s85\n"
    "s_add_u32 s84, s84, s86\n"
    "s_add_u32 s78, s78, 4150755663\n"
    "s_addc_u32 s79, s84, 335903614\n"
    "v_mov_b32 v30, s78\n"
    "v_mov_b32 v31, s79\n"
    "v_mov_b32 v32, s78\n"
    "v_mov_b32 v33, s79\n"
    "s_add_u32 s94, s94, 128\n"
    "s_cmp_eq_u32 s94, 896\n"
    "s_cselect_b32 s94, 0, s94\n"
    "v_mfma_f32_32x32x16_bf16 v[160:175], v[212:215], a[100:103], v[160:175]\n"
    "buffer_load_dwordx4 v49, s[24:27], s95 offen offset:2048 lds\n"
    "s_add_u32 m0, s31, 110080\n"
    "v_mfma_f32_32x32x16_bf16 v[160:175], v[216:219], a[104:107], v[160:175]\n"
    "buffer_load_dwordx4 v50, s[24:27], s95 offen offset:0 lds\n"
    "s_add_u32 m0, s38, 105984\n"
    "v_mfma_f32_32x32x16_bf16 v[160:175], v[220:223], a[108:111], v[160:175]\n"
    "s_mov_b64 exec, s[82:83]\n"
    "buffer_load_dwordx4 v51, s[16:19], s95 offen offset:0 lds\n"
    "s_mov_b64 exec, -1\n"
    "s_nop 11\n"
    "v_cmp_eq_u32 vcc, 1, v3\n"
    "v_pk_mul_f32 v[160:161], v[90:91], v[160:161] op_sel_hi:[0,1]\n"
    "v_pk_mul_f32 v[162:163], v[90:91], v[162:163] op_sel_hi:[0,1]\n"
    "v_cvt_pk_bf16_f32 v240, v160, v161\n"
    "v_pk_mul_f32 v[164:165], v[90:91], v[164:165] op_sel_hi:[0,1]\n"
    "v_cvt_pk_bf16_f32 v241, v162, v163\n"
    "v_pk_mul_f32 v[166:167], v[90:91], v[166:167] op_sel_hi:[0,1]\n"
    "v_cvt_pk_bf16_f32 v242, v164, v165\n"
    "v_pk_mul_f32 v[168:169], v[90:91], v[168:169] op_sel_hi:[0,1]\n"
    "v_cvt_pk_bf16_f32 v243, v166, v167\n"
    "v_pk_mul_f32 v[170:171], v[90:91], v[170:171] op_sel_hi:[0,1]\n"
    "v_cvt_pk_bf16_f32 v244, v168, v169\n"
    "v_pk_mul_f32 v[172:173], v[90:91], v[172:173] op_sel_hi:[0,1]\n"
    "v_cvt_pk_bf16_f32 v245, v170, v171\n"
    "v_pk_mul_f32 v[174:175], v[90:91], v[174:175] op_sel_hi:[0,1]\n"
    "v_cvt_pk_bf16_f32 v246, v172, v173\n"
    "v_cvt_pk_bf16_f32 v247, v174, v175\n"
    "s_cmp_eq_u32 s75, 7\n"
    "s_cbranch_scc1 LpubKV_%=\n"
    "buffer_store_dwordx2 v[240:241], v84, s[48:51], s60 offen offset:0 sc1\n"
    "buffer_store_dwordx2 v[242:243], v84, s[48:51], s60 offen offset:512 sc1\n"
    "buffer_store_dwordx2 v[244:245], v84, s[48:51], s60 offen offset:1024 sc1\n"
    "buffer_store_dwordx2 v[246:247], v84, s[48:51], s60 offen offset:1536 sc1\n"
    "s_branch LpubE_%=\n"
    "LpubKV_%=:\n"
    "s_cmp_eq_u32 s74, 0\n"
    "s_cbranch_scc0 LpubV_%=\n"
    "v_cndmask_b32 v246, v246, 0, vcc\n"
    "v_cndmask_b32 v247, v247, 0, vcc\n"
    "s_branch LpubS_%=\n"
    "LpubV_%=:\n"
    "ds_write_b128 v85, v[240:243]\n"
    "v_xor_b32 v85, 32, v85\n"
    "ds_write_b128 v85, v[244:247]\n"
    "s_waitcnt lgkmcnt(0)\n"
    "ds_read_b64_tr_b16 v[248:249], v86 offset:0\n"
    "ds_read_b64_tr_b16 v[250:251], v86 offset:512\n"
    "ds_read_b64_tr_b16 v[252:253], v86 offset:1024\n"
    "ds_read_b64_tr_b16 v[254:255], v86 offset:1536\n"
    "s_waitcnt lgkmcnt(0)\n"
    "v_and_or_b32 v240, v248, v87, v88\n"
    "v_and_or_b32 v241, v249, v87, v88\n"
    "v_and_or_b32 v242, v250, v87, v88\n"
    "v_and_or_b32 v243, v251, v87, v88\n"
    "v_and_or_b32 v244, v252, v87, v88\n"
    "v_and_or_b32 v245, v253, v87, v88\n"
    "v_and_or_b32 v246, v254, v87, v88\n"
    "v_and_or_b32 v247, v255, v87, v88\n"
    "LpubS_%=:\n"
    "v_mov_b32 v85, 2147483648\n"
    "buffer_store_dwordx4 v[240:243], v4, s[48:51], s60 offen offset:0 sc1\n"
    "buffer_store_dwordx4 v[244:247], v4, s[48:51], s60 offen offset:1024 sc1\n"
    "buffer_store_dword v85, v85, s[48:51], 0 offen\n"
    "buffer_store_dword v85, v85, s[48:51], 0 offen\n"
    "LpubE_%=:\n"
    "s_waitcnt vmcnt(16)\n"
    "s_barrier\n"
    "ds_read_b128 v[176:179], v39 offset:0\n"
    "ds_read_b128 v[180:183], v39 offset:7296\n"
    "ds_read_b128 v[184:187], v40 offset:0\n"
    "ds_read_b128 v[188:191], v40 offset:7296\n"
    "ds_read_b128 v[192:195], v41 offset:0\n"
    "ds_read_b128 v[196:199], v41 offset:7296\n"
    "ds_read_b128 v[200:203], v42 offset:0\n"
    "ds_read_b128 v[204:207], v42 offset:7296\n"
    "s_waitcnt vmcnt(12)\n"
    "s_barrier\n"
    "s_add_u32 m0, s31, 120448\n"
    "s_add_u32 s95, s94, s30\n"
    "s_waitcnt lgkmcnt(6)\n"
    "v_mfma_f32_32x32x16_bf16 v[128:143], a[0:3], v[176:179], 0\n"
    "ds_read_b128 v[208:211], v39 offset:14464\n"
    "buffer_load_dwordx4 v48, s[24:27], s95 offen offset:0 lds\n"
    "v_lshlrev_b32 v29, 16, v29\n"
    "v_mfma_f32_32x32x16_bf16 v[144:159], a[0:3], v[180:183], 0\n"
    "ds_read_b128 v[212:215], v39 offset:21760\n"
    "v_lshlrev_b32 v27, 16, v27\n"
    "v_mul_f32 v29, 1069066811, v29\n"
    "v_exp_f32 v29, v29\n"
    "v_lshlrev_b32 v28, 16, v28\n"
    "s_add_u32 s94, s94, 128\n"
    "s_cmp_eq_u32 s94, 896\n"
    "s_cselect_b32 s94, 0, s94\n"
    "s_waitcnt lgkmcnt(6)\n"
    "v_mfma_f32_32x32x16_bf16 v[128:143], a[4:7], v[184:187], v[128:143]\n"
    "ds_read_b128 v[216:219], v40 offset:14464\n"
    "buffer_load_dwordx4 v49, s[24:27], s95 offen offset:2048 lds\n"
    "s_add_u32 m0, s31, 124544\n"
    "v_cvt_pk_bf16_f32 v29, v29, v29\n"
    "v_mfma_f32_32x32x16_bf16 v[144:159], a[4:7], v[188:191], v[144:159]\n"
    "ds_read_b128 v[220:223], v40 offset:21760\n"
    "v_and_b32 v29, 0xffff0000, v29\n"
    "v_mul_f32 v5, v29, v27\n"
    "v_mul_f32 v6, v29, v28\n"
    "v_mul_f32 v5, 1069066811, v5\n"
    "s_waitcnt lgkmcnt(6)\n"
    "v_mfma_f32_32x32x16_bf16 v[128:143], a[8:11], v[192:195], v[128:143]\n"
    "ds_read_b128 v[224:227], v41 offset:14464\n"
    "buffer_load_dwordx4 v50, s[24:27], s95 offen offset:0 lds\n"
    "s_add_u32 m0, s38, 120448\n"
    "v_mul_f32 v6, 1042479491, v6\n"
    "v_mfma_f32_32x32x16_bf16 v[144:159], a[8:11], v[196:199], v[144:159]\n"
    "ds_read_b128 v[228:231], v41 offset:21760\n"
    "v_cmp_lt_f32 vcc, |v6|, 1.0\n"
    "v_fract_f32 v7, v6\n"
    "v_exp_f32 v5, v5\n"
    "v_mul_f32 v9, v27, v27\n"
    "s_waitcnt lgkmcnt(6)\n"
    "v_mfma_f32_32x32x16_bf16 v[128:143], a[12:15], v[200:203], v[128:143]\n"
    "ds_read_b128 v[232:235], v42 offset:14464\n"
    "s_mov_b64 exec, s[82:83]\n"
    "buffer_load_dwordx4 v51, s[16:19], s95 offen offset:0 lds\n"
    "s_mov_b64 exec, -1\n"
    "v_cndmask_b32 v6, v7, v6, vcc\n"
    "v_mfma_f32_32x32x16_bf16 v[144:159], a[12:15], v[204:207], v[144:159]\n"
    "ds_read_b128 v[236:239], v42 offset:21760\n"
    "v_cos_f32 v7, v6\n"
    "v_sin_f32 v8, v6\n"
    "v_fmac_f32 v9, v28, v28\n"
    "v_mul_f32 v102, v5, v7\n"
    "s_waitcnt vmcnt(4)\n"
    "s_waitcnt vmcnt(12)\n"
    "s_barrier\n"
    "buffer_store_dwordx4 v[30:33], v98, s[48:51], s61 offen sc1\n"
    "s_add_u32 m0, s31, 134912\n"
    "s_add_u32 s95, s94, s30\n"
    "s_waitcnt lgkmcnt(6)\n"
    "v_mfma_f32_32x32x16_bf16 v[128:143], a[16:19], v[208:211], v[128:143]\n"
    "ds_read_b128 v[176:179], v39 offset:28928\n"
    "buffer_load_dwordx4 v48, s[24:27], s95 offen offset:0 lds\n"
    "v_mul_f32 v103, v5, v8\n"
    "v_mfma_f32_32x32x16_bf16 v[144:159], a[16:19], v[212:215], v[144:159]\n"
    "ds_read_b128 v[180:183], v39 offset:36224\n"
    "v_rcp_f32 v5, v9\n"
    "v_add_f32 v6, -1.0, v102\n"
    "v_mul_f32 v7, v6, v27\n"
    "v_mul_f32 v8, v103, v27\n"
    "s_add_u32 s94, s94, 128\n"
    "s_cmp_eq_u32 s94, 896\n"
    "s_cselect_b32 s94, 0, s94\n"
    "s_waitcnt lgkmcnt(6)\n"
    "v_mfma_f32_32x32x16_bf16 v[128:143], a[20:23], v[216:219], v[128:143]\n"
    "ds_read_b128 v[184:187], v40 offset:28928\n"
    "buffer_load_dwordx4 v49, s[24:27], s95 offen offset:2048 lds\n"
    "s_add_u32 m0, s31, 139008\n"
    "v_add_f32 v5, v5, v5\n"
    "v_mfma_f32_32x32x16_bf16 v[144:159], a[20:23], v[220:223], v[144:159]\n"
    "ds_read_b128 v[188:191], v40 offset:36224\n"
    "v_fmac_f32 v7, v103, v28\n"
    "v_fma_f32 v8, -v6, v28, v8\n"
    "v_mul_f32 v114, v5, v7\n"
    "v_mul_f32 v115, v5, v8\n"
    "s_waitcnt lgkmcnt(6)\n"
    "v_mfma_f32_32x32x16_bf16 v[128:143], a[24:27], v[224:227], v[128:143]\n"
    "ds_read_b128 v[192:195], v41 offset:28928\n"
    "buffer_load_dwordx4 v50, s[24:27], s95 offen offset:0 lds\n"
    "s_add_u32 m0, s38, 134912\n"
    "v_mul_f32 v120, v102, v102\n"
    "v_mfma_f32_32x32x16_bf16 v[144:159], a[24:27], v[228:231], v[144:159]\n"
    "ds_read_b128 v[196:199], v41 offset:36224\n"
    "v_mul_f32 v121, v102, v103\n"
    "v_fma_f32 v120, -v103, v103, v120\n"
    "v_add_f32 v121, v121, v121\n"
    "v_mul_f32 v122, v120, v120\n"
    "s_waitcnt lgkmcnt(6)\n"
    "v_mfma_f32_32x32x16_bf16 v[128:143], a[28:31], v[232:235], v[128:143]\n"
    "ds_read_b128 v[200:203], v42 offset:28928\n"
    "s_mov_b64 exec, s[82:83]\n"
    "buffer_load_dwordx4 v51, s[16:19], s95 offen offset:0 lds\n"
    "s_mov_b64 exec, -1\n"
    "v_mul_f32 v123, v120, v121\n"
    "v_mfma_f32_32x32x16_bf16 v[144:159], a[28:31], v[236:239], v[144:159]\n"
    "ds_read_b128 v[204:207], v42 offset:36224\n"
    "v_fma_f32 v122, -v121, v121, v122\n"
    "v_add_f32 v123, v123, v123\n"
    "v_mul_f32 v104, v122, v122\n"
    "v_mul_f32 v105, v122, v123\n"
    "s_waitcnt vmcnt(13)\n"
    "s_barrier\n"
    "v_add_u32 v39, 48640, v39\n"
    "v_add_u32 v40, 48640, v40\n"
    "v_add_u32 v41, 48640, v41\n"
    "v_add_u32 v42, 48640, v42\n"
    "s_add_u32 m0, s31, 149376\n"
    "s_add_u32 s95, s94, s30\n"
    "s_waitcnt lgkmcnt(6)\n"
    "v_mfma_f32_32x32x16_bf16 v[128:143], a[32:35], v[176:179], v[128:143]\n"
    "ds_read_b128 v[208:211], v39 offset:0\n"
    "buffer_load_dwordx4 v48, s[24:27], s95 offen offset:0 lds\n"
    "v_fma_f32 v104, -v123, v123, v104\n"
    "v_mfma_f32_32x32x16_bf16 v[144:159], a[32:35], v[180:183], v[144:159]\n"
    "ds_read_b128 v[212:215], v39 offset:7296\n"
    "v_add_f32 v105, v105, v105\n"
    "v_mul_f32 v106, v104, v104\n"
    "v_mul_f32 v107, v104, v105\n"
    "v_fma_f32 v106, -v105, v105, v106\n"
    "s_add_u32 s94, s94, 128\n"
    "s_cmp_eq_u32 s94, 896\n"
    "s_cselect_b32 s94, 0, s94\n"
    "s_waitcnt lgkmcnt(6)\n"
    "v_mfma_f32_32x32x16_bf16 v[128:143], a[36:39], v[184:187], v[128:143]\n"
    "ds_read_b128 v[216:219], v40 offset:0\n"
    "buffer_load_dwordx4 v49, s[24:27], s95 offen offset:2048 lds\n"
    "s_add_u32 m0, s31, 153472\n"
    "v_add_f32 v107, v107, v107\n"
    "v_mfma_f32_32x32x16_bf16 v[144:159], a[36:39], v[188:191], v[144:159]\n"
    "ds_read_b128 v[220:223], v40 offset:7296\n"
    "v_mul_f32 v124, v106, v106\n"
    "v_mul_f32 v125, v106, v107\n"
    "v_fma_f32 v124, -v107, v107, v124\n"
    "v_add_f32 v125, v125, v125\n"
    "s_waitcnt lgkmcnt(6)\n"
    "v_mfma_f32_32x32x16_bf16 v[128:143], a[40:43], v[192:195], v[128:143]\n"
    "ds_read_b128 v[224:227], v41 offset:0\n"
    "buffer_load_dwordx4 v50, s[24:27], s95 offen offset:0 lds\n"
    "s_add_u32 m0, s38, 149376\n"
    "v_mul_f32 v108, v124, v124\n"
    "v_mfma_f32_32x32x16_bf16 v[144:159], a[40:43], v[196:199], v[144:159]\n"
    "ds_read_b128 v[228:231], v41 offset:7296\n"
    "v_mul_f32 v109, v124, v125\n"
    "v_fma_f32 v108, -v125, v125, v108\n"
    "v_add_f32 v109, v109, v109\n"
    "v_mul_f32 v120, v108, v108\n"
    "s_waitcnt lgkmcnt(6)\n"
    "v_mfma_f32_32x32x16_bf16 v[128:143], a[44:47], v[200:203], v[128:143]\n"
    "ds_read_b128 v[232:235], v42 offset:0\n"
    "s_mov_b64 exec, s[82:83]\n"
    "buffer_load_dwordx4 v51, s[16:19], s95 offen offset:0 lds\n"
    "s_mov_b64 exec, -1\n"
    "v_mul_f32 v121, v108, v109\n"
    "v_mfma_f32_32x32x16_bf16 v[144:159], a[44:47], v[204:207], v[144:159]\n"
    "ds_read_b128 v[236:239], v42 offset:7296\n"
    "v_fma_f32 v120, -v109, v109, v120\n"
    "v_add_f32 v121, v121, v121\n"
    "v_mul_f32 v122, v120, v120\n"
    "v_mul_f32 v123, v120, v121\n"
    "s_waitcnt vmcnt(9)\n"
    "s_barrier\n"
    "s_waitcnt lgkmcnt(6)\n"
    "v_mfma_f32_32x32x16_bf16 v[128:143], a[48:51], v[208:211], v[128:143]\n"
    "ds_read_b128 v[176:179], v39 offset:14464\n"
    "v_fma_f32 v122, -v121, v121, v122\n"
    "v_add_f32 v123, v123, v123\n"
    "v_mul_f32 v110, v122, v122\n"
    "v_mul_f32 v111, v122, v123\n"
    "v_mfma_f32_32x32x16_bf16 v[144:159], a[48:51], v[212:215], v[144:159]\n"
    "ds_read_b128 v[180:183], v39 offset:21760\n"
    "v_fma_f32 v110, -v123, v123, v110\n"
    "v_add_f32 v111, v111, v111\n"
    "v_mul_f32 v112, v110, v110\n"
    "v_mul_f32 v113, v110, v111\n"
    "s_waitcnt lgkmcnt(6)\n"
    "v_mfma_f32_32x32x16_bf16 v[128:143], a[52:55], v[216:219], v[128:143]\n"
    "ds_read_b128 v[184:187], v40 offset:14464\n"
    "v_fma_f32 v112, -v111, v111, v112\n"
    "v_add_f32 v113, v113, v113\n"
    "v_mul_f32 v126, v124, v106\n"
    "v_mul_f32 v127, v124, v107\n"
    "v_mfma_f32_32x32x16_bf16 v[144:159], a[52:55], v[220:223], v[144:159]\n"
    "ds_read_b128 v[188:191], v40 offset:21760\n"
    "v_fma_f32 v126, -v125, v107, v126\n"
    "v_fmac_f32 v127, v125, v106\n"
    "v_cmp_eq_u32 vcc, 1, v3\n"
    "v_cmp_eq_u32 s[80:81], 1, v53\n"
    "s_waitcnt lgkmcnt(6)\n"
    "v_mfma_f32_32x32x16_bf16 v[128:143], a[56:59], v[224:227], v[128:143]\n"
    "ds_read_b128 v[192:195], v41 offset:14464\n"
    "v_mov_b32 v7, 0\n"
    "v_cndmask_b32 v5, 1.0, v106, vcc\n"
    "v_cndmask_b32 v6, v124, v126, vcc\n"
    "v_cndmask_b32 v118, v5, v6, s[80:81]\n"
    "v_mfma_f32_32x32x16_bf16 v[144:159], a[56:59], v[228:231], v[144:159]\n"
    "ds_read_b128 v[196:199], v41 offset:21760\n"
    "v_cndmask_b32 v5, 0, v107, vcc\n"
    "v_cndmask_b32 v6, v125, v127, vcc\n"
    "v_cndmask_b32 v119, v5, v6, s[80:81]\n"
    "s_waitcnt lgkmcnt(6)\n"
    "v_mfma_f32_32x32x16_bf16 v[128:143], a[60:63], v[232:235], v[128:143]\n"
    "ds_read_b128 v[200:203], v42 offset:14464\n"
    "s_lshl_b32 s84, s74, 8\n"
    "s_add_u32 s84, s84, 101760\n"
    "v_add_u32 v56, s84, v97\n"
    "v_lshl_add_u32 v55, v3, 9, v56\n"
    "v_mfma_f32_32x32x16_bf16 v[144:159], a[60:63], v[236:239], v[144:159]\n"
    "ds_read_b128 v[204:207], v42 offset:21760\n"
    "v_lshl_add_u32 v55, v53, 10, v55\n"
    "s_lshl_b32 s84, s73, 7\n"
    "s_add_u32 s84, s84, 103808\n"
    "v_lshl_add_u32 v54, v3, 4, s84\n"
    "s_waitcnt vmcnt(4)\n"
    "s_barrier\n"
    "s_waitcnt lgkmcnt(6)\n"
    "v_mfma_f32_32x32x16_bf16 v[128:143], a[64:67], v[176:179], v[128:143]\n"
    "ds_read_b128 v[208:211], v39 offset:28928\n"
    "v_lshlrev_b32 v57, 7, v2\n"
    "v_cmp_gt_u32 vcc, s76, v2\n"
    "v_mov_b32 v6, 2147483648\n"
    "s_nop 0\n"
    "v_mfma_f32_32x32x16_bf16 v[144:159], a[64:67], v[180:183], v[144:159]\n"
    "ds_read_b128 v[212:215], v39 offset:36224\n"
    "v_cndmask_b32 v95, v6, v57, vcc\n"
    "v_or_b32 v6, v3, v53\n"
    "v_lshl_or_b32 v69, v6, 31, v97\n"
    "v_lshl_or_b32 v70, v53, 31, v52\n"
    "s_waitcnt lgkmcnt(6)\n"
    "v_mfma_f32_32x32x16_bf16 v[128:143], a[68:71], v[184:187], v[128:143]\n"
    "ds_read_b128 v[216:219], v40 offset:28928\n"
    "s_lshl_b32 s29, s75, 5\n"
    "s_add_u32 s29, s29, 31\n"
    "s_sub_u32 s29, s29, s76\n"
    "s_lshl1_add_u32 s29, s29, s74\n"
    "v_mfma_f32_32x32x16_bf16 v[144:159], a[68:71], v[188:191], v[144:159]\n"
    "ds_read_b128 v[220:223], v40 offset:36224\n"
    "s_lshl_b32 s29, s29, 8\n"
    "s_add_u32 s29, s29, 32768\n"
    "s_xor_b32 s84, s73, 1\n"
    "s_lshl3_add_u32 s84, s84, s75\n"
    "s_waitcnt lgkmcnt(6)\n"
    "v_mfma_f32_32x32x16_bf16 v[128:143], a[72:75], v[192:195], v[128:143]\n"
    "ds_read_b128 v[224:227], v41 offset:28928\n"
    "s_lshl_b32 s84, s84, 1\n"
    "v_and_b32 v5, 1, v1\n"
    "v_add_u32 v5, s84, v5\n"
    "v_and_b32 v6, 15, v1\n"
    "v_mfma_f32_32x32x16_bf16 v[144:159], a[72:75], v[196:199], v[144:159]\n"
    "ds_read_b128 v[228:231], v41 offset:36224\n"
    "v_cmp_gt_u32 vcc, 16, v1\n"
    "v_lshl_add_u32 v6, v53, 4, v6\n"
    "s_nop 0\n"
    "v_cndmask_b32 v5, v5, v6, vcc\n"
    "s_waitcnt lgkmcnt(6)\n"
    "v_mfma_f32_32x32x16_bf16 v[128:143], a[76:79], v[200:203], v[128:143]\n"
    "ds_read_b128 v[232:235], v42 offset:28928\n"
    "v_lshlrev_b32 v96, 7, v5\n"
    "v_add_u32 v75, 0, v46\n"
    "v_add_u32 v76, 6144, v47\n"
    "v_add_u32 v77, 12288, v46\n"
    "v_mfma_f32_32x32x16_bf16 v[144:159], a[76:79], v[204:207], v[144:159]\n"
    "ds_read_b128 v[236:239], v42 offset:36224\n"
    "v_add_u32 v78, 18432, v47\n"
    "v_add_u32 v79, 28672, v46\n"
    "v_add_u32 v80, 34816, v47\n"
    "v_add_u32 v81, 40960, v46\n"
    "s_mov_b32 s87, 8912896\n"
    "buffer_load_dwordx2 v[60:61], v58, s[48:51], s87 offen sc1\n"
    "s_waitcnt vmcnt(1)\n"
    "s_barrier\n"
    "s_waitcnt lgkmcnt(6)\n"
    "v_mfma_f32_32x32x16_bf16 v[128:143], a[80:83], v[208:211], v[128:143]\n"
    "ds_read_b128 v[176:179], v39 offset:43392\n"
    "v_lshlrev_b32 v5, 2, v23\n"
    "v_xor_b32 v5, v17, v5\n"
    "v_min_u32 v5, 6, v5\n"
    "v_mul_u32_u24 v6, 896, v16\n"
    "v_mfma_f32_32x32x16_bf16 v[144:159], a[80:83], v[212:215], v[144:159]\n"
    "ds_read_b128 v[180:183], v39 offset:50688\n"
    "v_lshl_add_u32 v89, v5, 4, v6\n"
    "v_add_u32 v92, 6144, v89\n"
    "v_add_u32 v93, 12288, v89\n"
    "v_add_u32 v94, 18432, v89\n"
    "s_waitcnt lgkmcnt(6)\n"
    "v_mfma_f32_32x32x16_bf16 v[128:143], a[84:87], v[216:219], v[128:143]\n"
    "ds_read_b128 v[184:187], v40 offset:43392\n"
    "s_sub_u32 s84, 32, s76\n"
    "s_lshl1_add_u32 s84, s84, s74\n"
    "s_lshl_b32 s84, s84, 8\n"
    "v_lshl_add_u32 v6, v3, 13, v97\n"
    "v_mfma_f32_32x32x16_bf16 v[144:159], a[84:87], v[220:223], v[144:159]\n"
    "ds_read_b128 v[188:191], v40 offset:50688\n"
    "v_add_u32 v59, s84, v6\n"
    "v_add_u32 v62, 4096, v59\n"
    "s_lshl_b32 s84, s74, 31\n"
    "v_or_b32 v65, s84, v4\n"
    "s_waitcnt lgkmcnt(6)\n"
    "v_mfma_f32_32x32x16_bf16 v[128:143], a[88:91], v[224:227], v[128:143]\n"
    "ds_read_b128 v[192:195], v41 offset:43392\n"
    "s_lshl_b32 s84, s72, 12\n"
    "s_add_u32 s84, s84, 57344\n"
    "v_lshl_add_u32 v5, v2, 6, s84\n"
    "v_bfe_u32 v6, v2, 1, 2\n"
    "v_mfma_f32_32x32x16_bf16 v[144:159], a[88:91], v[228:231], v[144:159]\n"
    "ds_read_b128 v[196:199], v41 offset:50688\n"
    "v_lshlrev_b32 v7, 1, v3\n"
    "v_xor_b32 v6, v6, v7\n"
    "v_lshl_add_u32 v63, v6, 4, v5\n"
    "v_xor_b32 v99, 16, v63\n"
    "s_waitcnt lgkmcnt(6)\n"
    "v_mfma_f32_32x32x16_bf16 v[128:143], a[92:95], v[232:235], v[128:143]\n"
    "ds_read_b128 v[200:203], v42 offset:43392\n"
    "v_and_b32 v6, 1, v22\n"
    "v_lshrrev_b32 v7, 1, v22\n"
    "v_lshl_or_b32 v6, v6, 1, v23\n"
    "v_lshrrev_b32 v8, 1, v21\n"
    "v_mfma_f32_32x32x16_bf16 v[144:159], a[92:95], v[236:239], v[144:159]\n"
    "ds_read_b128 v[204:207], v42 offset:50688\n"
    "v_xor_b32 v6, v6, v8\n"
    "v_lshl_add_u32 v6, v6, 4, s84\n"
    "v_lshl_add_u32 v6, v7, 3, v6\n"
    "v_lshl_add_u32 v6, v21, 6, v6\n"
    "s_waitcnt lgkmcnt(6)\n"
    "v_mfma_f32_32x32x16_bf16 v[128:143], a[96:99], v[176:179], v[128:143]\n"
    "v_lshl_add_u32 v64, v3, 11, v6\n"
    "v_xor_b32 v100, 32, v64\n"
    "v_bfe_u32 v5, v2, 2, 1\n"
    "v_and_b32 v6, 1, v2\n"
    "v_mfma_f32_32x32x16_bf16 v[144:159], a[96:99], v[180:183], v[144:159]\n"
    "v_lshrrev_b32 v7, 3, v2\n"
    "v_bfe_u32 v8, v2, 1, 1\n"
    "v_lshl_or_b32 v8, v5, 4, v8\n"
    "v_lshl_or_b32 v8, v6, 3, v8\n"
    "s_waitcnt lgkmcnt(4)\n"
    "v_mfma_f32_32x32x16_bf16 v[128:143], a[100:103], v[184:187], v[128:143]\n"
    "v_lshl_or_b32 v8, v7, 1, v8\n"
    "v_lshl_add_u32 v66, v53, 5, v8\n"
    "v_mul_u32_u24 v5, 896, v66\n"
    "v_lshlrev_b32 v68, 3, v3\n"
    "v_mfma_f32_32x32x16_bf16 v[144:159], a[100:103], v[188:191], v[144:159]\n"
    "v_lshl_add_u32 v67, v3, 5, v5\n"
    "s_waitcnt lgkmcnt(2)\n"
    "v_mfma_f32_32x32x16_bf16 v[128:143], a[104:107], v[192:195], v[128:143]\n"
    "v_mfma_f32_32x32x16_bf16 v[144:159], a[104:107], v[196:199], v[144:159]\n"
    "s_waitcnt lgkmcnt(0)\n"
    "v_mfma_f32_32x32x16_bf16 v[128:143], a[108:111], v[200:203], v[128:143]\n"
    "v_mfma_f32_32x32x16_bf16 v[144:159], a[108:111], v[204:207], v[144:159]\n"
    "s_waitcnt vmcnt(0)\n"
    "v_cmp_ne_u64 vcc, s[78:79], v[60:61]\n"
    "s_nop 0\n"
    "s_and_b64 vcc, vcc, -1\n"
    "s_cbranch_scc1 Lkvsl_%=\n"
    "Lkvok_%=:\n"
    "global_load_dwordx4 a[112:115], v4, s[36:37] offset:-4096 sc1\n"
    "global_load_dwordx4 a[116:119], v4, s[36:37] offset:-3072 sc1\n"
    "s_cmp_eq_u32 s74, 1\n"
    "s_cselect_b32 s85, 0x01000000, 0\n"
    "s_nop 7\n"
    "s_cmp_eq_u32 s74, 1\n"
    "s_cbranch_scc0 Lnog_%=\n"
    "s_mov_b32 exec_lo, s85\n"
    "s_mov_b32 exec_hi, s85\n"
    "ds_write_b128 v54, v[128:131] offset:0\n"
    "ds_write_b128 v54, v[132:135] offset:32\n"
    "ds_write_b128 v54, v[136:139] offset:64\n"
    "ds_write_b128 v54, v[140:143] offset:96\n"
    "s_mov_b64 exec, -1\n"
    "Lnog_%=:\n"
    "v_pk_mul_f32 v[40:41], v[114:115], v[128:129] op_sel_hi:[0,1]\n"
    "v_pk_mul_f32 v[42:43], v[114:115], v[144:145] op_sel_hi:[0,1]\n"
    "v_pk_fma_f32 v[144:145], v[114:115], v[144:145], v[40:41] op_sel:[1,0,0] op_sel_hi:[1,1,1] neg_lo:[1,0,0] neg_hi:[1,0,0]\n"
    "v_pk_fma_f32 v[128:129], v[114:115], v[128:129], v[42:43] op_sel:[1,0,0] op_sel_hi:[1,1,1]\n"
    "buffer_load_dwordx2 a[152:153], v82, s[48:51], s67 offen offset:0 sc1\n"
    "v_pk_mul_f32 v[116:117], v[114:115], v[130:131] op_sel_hi:[0,1]\n"
    "v_pk_mul_f32 v[18:19], v[114:115], v[146:147] op_sel_hi:[0,1]\n"
    "v_pk_fma_f32 v[146:147], v[114:115], v[146:147], v[116:117] op_sel:[1,0,0] op_sel_hi:[1,1,1] neg_lo:[1,0,0] neg_hi:[1,0,0]\n"
    "v_pk_fma_f32 v[130:131], v[114:115], v[130:131], v[18:19] op_sel:[1,0,0] op_sel_hi:[1,1,1]\n"
    "buffer_load_dwordx2 a[154:155], v82, s[48:51], s67 offen offset:256 sc1\n"
    "v_pk_fma_f32 v[116:117], v[102:103], v[144:145], v[146:147] op_sel_hi:[0,1,1]\n"
    "v_pk_mul_f32 v[40:41], v[114:115], v[132:133] op_sel_hi:[0,1]\n"
    "v_pk_fma_f32 v[18:19], v[102:103], v[128:129], v[130:131] op_sel_hi:[0,1,1]\n"
    "v_pk_mul_f32 v[42:43], v[114:115], v[148:149] op_sel_hi:[0,1]\n"
    "buffer_load_dwordx2 a[156:157], v82, s[48:51], s67 offen offset:1024 sc1\n"
    "v_pk_fma_f32 v[36:37], v[102:103], v[128:129], v[116:117] op_sel:[1,0,0] op_sel_hi:[1,1,1] neg_lo:[1,0,0] neg_hi:[1,0,0]\n"
    "v_pk_fma_f32 v[148:149], v[114:115], v[148:149], v[40:41] op_sel:[1,0,0] op_sel_hi:[1,1,1] neg_lo:[1,0,0] neg_hi:[1,0,0]\n"
    "v_pk_fma_f32 v[38:39], v[102:103], v[144:145], v[18:19] op_sel:[1,0,0] op_sel_hi:[1,1,1]\n"
    "v_pk_fma_f32 v[132:133], v[114:115], v[132:133], v[42:43] op_sel:[1,0,0] op_sel_hi:[1,1,1]\n"
    "buffer_load_dwordx2 a[158:159], v83, s[48:51], s67 offen offset:1280 sc1\n"
    "v_pk_fma_f32 v[116:117], v[102:103], v[36:37], v[148:149] op_sel_hi:[0,1,1]\n"
    "v_pk_mul_f32 v[40:41], v[114:115], v[134:135] op_sel_hi:[0,1]\n"
    "v_pk_fma_f32 v[18:19], v[102:103], v[38:39], v[132:133] op_sel_hi:[0,1,1]\n"
    "v_pk_mul_f32 v[42:43], v[114:115], v[150:151] op_sel_hi:[0,1]\n"
    "buffer_load_dwordx2 a[160:161], v82, s[48:51], s77 offen offset:0 sc1\n"
    "v_pk_fma_f32 v[24:25], v[102:103], v[38:39], v[116:117] op_sel:[1,0,0] op_sel_hi:[1,1,1] neg_lo:[1,0,0] neg_hi:[1,0,0]\n"
    "v_pk_fma_f32 v[150:151], v[114:115], v[150:151], v[40:41] op_sel:[1,0,0] op_sel_hi:[1,1,1] neg_lo:[1,0,0] neg_hi:[1,0,0]\n"
    "v_pk_fma_f32 v[28:29], v[102:103], v[36:37], v[18:19] op_sel:[1,0,0] op_sel_hi:[1,1,1]\n"
    "v_pk_fma_f32 v[134:135], v[114:115], v[134:135], v[42:43] op_sel:[1,0,0] op_sel_hi:[1,1,1]\n"
    "buffer_load_dwordx2 a[162:163], v82, s[48:51], s77 offen offset:256 sc1\n"
    "v_pk_fma_f32 v[116:117], v[102:103], v[24:25], v[150:151] op_sel_hi:[0,1,1]\n"
    "v_pk_mul_f32 v[40:41], v[114:115], v[136:137] op_sel_hi:[0,1]\n"
    "v_pk_fma_f32 v[18:19], v[102:103], v[28:29], v[134:135] op_sel_hi:[0,1,1]\n"
    "v_pk_mul_f32 v[42:43], v[114:115], v[152:153] op_sel_hi:[0,1]\n"
    "buffer_load_dwordx2 a[164:165], v82, s[48:51], s77 offen offset:1024 sc1\n"
    "v_pk_fma_f32 v[36:37], v[102:103], v[28:29], v[116:117] op_sel:[1,0,0] op_sel_hi:[1,1,1] neg_lo:[1,0,0] neg_hi:[1,0,0]\n"
    "v_pk_fma_f32 v[152:153], v[114:115], v[152:153], v[40:41] op_sel:[1,0,0] op_sel_hi:[1,1,1] neg_lo:[1,0,0] neg_hi:[1,0,0]\n"
    "v_pk_fma_f32 v[38:39], v[102:103], v[24:25], v[18:19] op_sel:[1,0,0] op_sel_hi:[1,1,1]\n"
    "v_pk_fma_f32 v[136:137], v[114:115], v[136:137], v[42:43] op_sel:[1,0,0] op_sel_hi:[1,1,1]\n"
    "buffer_load_dwordx2 a[166:167], v83, s[48:51], s77 offen offset:1280 sc1\n"
    "v_pk_fma_f32 v[116:117], v[102:103], v[36:37], v[152:153] op_sel_hi:[0,1,1]\n"
    "v_pk_mul_f32 v[40:41], v[114:115], v[138:139] op_sel_hi:[0,1]\n"
    "v_pk_fma_f32 v[18:19], v[102:103], v[38:39], v[136:137] op_sel_hi:[0,1,1]\n"
    "v_pk_mul_f32 v[42:43], v[114:115], v[154:155] op_sel_hi:[0,1]\n"
    "global_load_dwordx4 a[120:123], v4, s[36:37] offset:-2048 sc1\n"
    "v_pk_fma_f32 v[24:25], v[102:103], v[38:39], v[116:117] op_sel:[1,0,0] op_sel_hi:[1,1,1] neg_lo:[1,0,0] neg_hi:[1,0,0]\n"
    "v_pk_fma_f32 v[154:155], v[114:115], v[154:155], v[40:41] op_sel:[1,0,0] op_sel_hi:[1,1,1] neg_lo:[1,0,0] neg_hi:[1,0,0]\n"
    "v_pk_fma_f32 v[28:29], v[102:103], v[36:37], v[18:19] op_sel:[1,0,0] op_sel_hi:[1,1,1]\n"
    "v_pk_fma_f32 v[138:139], v[114:115], v[138:139], v[42:43] op_sel:[1,0,0] op_sel_hi:[1,1,1]\n"
    "global_load_dwordx4 a[124:127], v4, s[36:37] offset:-1024 sc1\n"
    "v_pk_fma_f32 v[116:117], v[102:103], v[24:25], v[154:155] op_sel_hi:[0,1,1]\n"
    "v_pk_mul_f32 v[40:41], v[114:115], v[140:141] op_sel_hi:[0,1]\n"
    "v_pk_fma_f32 v[18:19], v[102:103], v[28:29], v[138:139] op_sel_hi:[0,1,1]\n"
    "v_pk_mul_f32 v[42:43], v[114:115], v[156:157] op_sel_hi:[0,1]\n"
    "buffer_load_dwordx2 a[168:169], v82, s[48:51], s88 offen offset:0 sc1\n"
    "v_pk_fma_f32 v[36:37], v[102:103], v[28:29], v[116:117] op_sel:[1,0,0] op_sel_hi:[1,1,1] neg_lo:[1,0,0] neg_hi:[1,0,0]\n"
    "v_pk_fma_f32 v[156:157], v[114:115], v[156:157], v[40:41] op_sel:[1,0,0] op_sel_hi:[1,1,1] neg_lo:[1,0,0] neg_hi:[1,0,0]\n"
    "v_pk_fma_f32 v[38:39], v[102:103], v[24:25], v[18:19] op_sel:[1,0,0] op_sel_hi:[1,1,1]\n"
    "v_pk_fma_f32 v[140:141], v[114:115], v[140:141], v[42:43] op_sel:[1,0,0] op_sel_hi:[1,1,1]\n"
    "buffer_load_dwordx2 a[170:171], v82, s[48:51], s88 offen offset:256 sc1\n"
    "v_pk_fma_f32 v[116:117], v[102:103], v[36:37], v[156:157] op_sel_hi:[0,1,1]\n"
    "v_pk_mul_f32 v[40:41], v[114:115], v[142:143] op_sel_hi:[0,1]\n"
    "v_pk_fma_f32 v[18:19], v[102:103], v[38:39], v[140:141] op_sel_hi:[0,1,1]\n"
    "v_pk_mul_f32 v[42:43], v[114:115], v[158:159] op_sel_hi:[0,1]\n"
    "buffer_load_dwordx2 a[172:173], v82, s[48:51], s88 offen offset:1024 sc1\n"
    "v_pk_fma_f32 v[24:25], v[102:103], v[38:39], v[116:117] op_sel:[1,0,0] op_sel_hi:[1,1,1] neg_lo:[1,0,0] neg_hi:[1,0,0]\n"
    "v_pk_fma_f32 v[158:159], v[114:115], v[158:159], v[40:41] op_sel:[1,0,0] op_sel_hi:[1,1,1] neg_lo:[1,0,0] neg_hi:[1,0,0]\n"
    "v_pk_fma_f32 v[28:29], v[102:103], v[36:37], v[18:19] op_sel:[1,0,0] op_sel_hi:[1,1,1]\n"
    "v_pk_fma_f32 v[142:143], v[114:115], v[142:143], v[42:43] op_sel:[1,0,0] op_sel_hi:[1,1,1]\n"
    "buffer_load_dwordx2 a[174:175], v83, s[48:51], s88 offen offset:1280 sc1\n"
    "v_pk_fma_f32 v[116:117], v[102:103], v[24:25], v[158:159] op_sel_hi:[0,1,1]\n"
    "v_pk_fma_f32 v[18:19], v[102:103], v[28:29], v[142:143] op_sel_hi:[0,1,1]\n"
    "v_pk_fma_f32 v[36:37], v[102:103], v[28:29], v[116:117] op_sel:[1,0,0] op_sel_hi:[1,1,1] neg_lo:[1,0,0] neg_hi:[1,0,0]\n"
    "v_pk_fma_f32 v[38:39], v[102:103], v[24:25], v[18:19] op_sel:[1,0,0] op_sel_hi:[1,1,1]\n"
    "buffer_load_dwordx2 a[144:145], v82, s[48:51], s66 offen offset:0 sc1\n"
    "v_pk_mov_b32 v[48:49], v[36:37], v[38:39] op_sel:[0,0]\n"
    "v_pk_mov_b32 v[40:41], v[36:37], v[38:39] op_sel:[1,1]\n"
    "v_pk_fma_f32 v[116:117], v[104:105], v[48:49], v[40:41] op_sel_hi:[0,1,1]\n"
    "v_pk_fma_f32 v[42:43], v[104:105], v[48:49], v[116:117] op_sel:[1,1,0] op_sel_hi:[1,0,1] neg_lo:[1,0,0]\n"
    "ds_write_b64 v55, v[42:43]\n"
    "v_lshlrev_b32 v7, 2, v2\n"
    "v_lshl_add_u32 v7, v53, 7, v7\n"
    "v_add_u32 v7, 103808, v7\n"
    "s_waitcnt lgkmcnt(0)\n"
    "s_barrier\n"
    "ds_read_b64 v[208:209], v56 offset:0\n"
    "ds_read_b64 v[210:211], v56 offset:512\n"
    "ds_read_b64 v[212:213], v56 offset:1024\n"
    "ds_read_b64 v[214:215], v56 offset:1536\n"
    "ds_read_b32 v15, v7\n"
    "buffer_load_dwordx2 a[146:147], v82, s[48:51], s66 offen offset:256 sc1\n"
    "buffer_load_dwordx2 a[148:149], v82, s[48:51], s66 offen offset:1024 sc1\n"
    "buffer_load_dwordx2 a[150:151], v83, s[48:51], s66 offen offset:1280 sc1\n"
    "global_load_dwordx4 a[128:131], v4, s[36:37] offset:0 sc1\n"
    "global_load_dwordx4 a[132:135], v4, s[36:37] offset:1024 sc1\n"
    "global_load_dwordx4 a[136:139], v4, s[36:37] offset:2048 sc1\n"
    "global_load_dwordx4 a[140:143], v4, s[36:37] offset:3072 sc1\n"
    "s_waitcnt lgkmcnt(0)\n"
    "v_cmp_eq_u32 vcc, 1, v3\n"
    "v_cmp_eq_u32 s[80:81], 1, v53\n"
    "v_mul_f32 v15, 3216550459, v15\n"
    "v_exp_f32 v15, v15\n"
    "v_pk_fma_f32 v[116:117], v[106:107], v[208:209], v[210:211] op_sel_hi:[0,1,1]\n"
    "v_pk_fma_f32 v[24:25], v[106:107], v[208:209], v[116:117] op_sel:[1,1,0] op_sel_hi:[1,0,1] neg_lo:[1,0,0]\n"
    "v_pk_fma_f32 v[116:117], v[106:107], v[24:25], v[212:213] op_sel_hi:[0,1,1]\n"
    "v_pk_fma_f32 v[28:29], v[106:107], v[24:25], v[116:117] op_sel:[1,1,0] op_sel_hi:[1,0,1] neg_lo:[1,0,0]\n"
    "v_pk_fma_f32 v[116:117], v[106:107], v[28:29], v[214:215] op_sel_hi:[0,1,1]\n"
    "v_pk_fma_f32 v[36:37], v[106:107], v[28:29], v[116:117] op_sel:[1,1,0] op_sel_hi:[1,0,1] neg_lo:[1,0,0]\n"
    "v_add_f32 v15, 1.0, v15\n"
    "v_rcp_f32 v15, v15\n"
    "v_cndmask_b32 v5, 0, v208, vcc\n"
    "v_cndmask_b32 v6, v24, v28, vcc\n"
    "v_cndmask_b32 v38, v5, v6, s[80:81]\n"
    "v_cndmask_b32 v5, 0, v209, vcc\n"
    "v_cndmask_b32 v6, v25, v29, vcc\n"
    "v_cndmask_b32 v39, v5, v6, s[80:81]\n"
    "v_mov_b32 v252, -1\n"
    "v_mov_b32 v253, -1\n"
    "v_mov_b32 v254, -1\n"
    "v_mov_b32 v255, -1\n"
    "buffer_store_dwordx2 v[36:37], v69, s[48:51], s29 offen sc1\n"
    "s_waitcnt vmcnt(15)\n"
    "v_mfma_f32_32x32x16_bf16 v[176:191], a[112:115], a[152:155], 0\n"
    "v_mfma_f32_32x32x16_bf16 v[176:191], a[116:119], a[156:159], v[176:191]\n"
    "s_lshl_b32 s39, s65, 12\n"
    "s_add_u32 s39, s39, 8257536\n"
    "s_lshl_b32 s44, s76, 7\n"
    "s_add_u32 s44, s44, s39\n"
    "s_lshl4_add_u32 s58, s64, s65\n"
    "s_lshl_b32 s57, s58, 11\n"
    "s_add_u32 s57, s57, 4194304\n"
    "s_mul_i32 s62, s75, 392\n"
    "s_add_u32 s62, s62, s73\n"
    "s_lshl_b32 s62, s62, 7\n"
    "s_lshl1_add_u32 s63, s73, s74\n"
    "s_mul_i32 s63, s63, 7168\n"
    "s_lshl_b32 s84, s75, 14\n"
    "s_add_u32 s84, s84, 32768\n"
    "s_add_u32 s20, s48, s84\n"
    "s_addc_u32 s21, s49, 0\n"
    "s_mov_b32 s22, 16384\n"
    "v_mfma_f32_32x32x16_bf16 v[192:207], a[112:115], a[160:163], 0\n"
    "s_add_u32 s36, s36, 8192\n"
    "s_addc_u32 s37, s37, 0\n"
    "v_exp_f32 v176, v176\n"
    "v_exp_f32 v177, v177\n"
    "v_exp_f32 v178, v178\n"
    "v_exp_f32 v179, v179\n"
    "v_cvt_pk_bf16_f32 v120, v176, v177\n"
    "s_nop 0\n"
    "global_load_dwordx4 a[176:179], v4, s[36:37] offset:-4096 sc1\n"
    "v_exp_f32 v180, v180\n"
    "v_exp_f32 v181, v181\n"
    "v_cvt_pk_bf16_f32 v121, v178, v179\n"
    "v_exp_f32 v182, v182\n"
    "v_exp_f32 v183, v183\n"
    "v_cvt_pk_bf16_f32 v122, v180, v181\n"
    "v_mfma_f32_32x32x16_bf16 v[192:207], a[116:119], a[164:167], v[192:207]\n"
    "s_waitcnt vmcnt(10)\n"
    "v_exp_f32 v184, v184\n"
    "v_exp_f32 v185, v185\n"
    "v_cvt_pk_bf16_f32 v123, v182, v183\n"
    "v_exp_f32 v186, v186\n"
    "v_exp_f32 v187, v187\n"
    "v_cvt_pk_bf16_f32 v124, v184, v185\n"
    "v_mfma_f32_32x32x16_bf16 a[208:223], a[120:123], v[120:123], 0\n"
    "global_load_dwordx4 a[180:183], v4, s[36:37] offset:-3072 sc1\n"
    "v_exp_f32 v188, v188\n"
    "v_exp_f32 v189, v189\n"
    "v_cvt_pk_bf16_f32 v125, v186, v187\n"
    "v_exp_f32 v190, v190\n"
    "v_exp_f32 v191, v191\n"
    "v_cvt_pk_bf16_f32 v126, v188, v189\n"
    "v_cvt_pk_bf16_f32 v127, v190, v191\n"
    "v_mfma_f32_32x32x16_bf16 v[176:191], a[112:115], a[168:171], 0\n"
    "s_mul_i32 s66, s72, 100352\n"
    "v_exp_f32 v192, v192\n"
    "v_exp_f32 v193, v193\n"
    "v_exp_f32 v194, v194\n"
    "v_exp_f32 v195, v195\n"
    "v_cvt_pk_bf16_f32 v120, v192, v193\n"
    "v_mfma_f32_32x32x16_bf16 a[208:223], a[124:127], v[124:127], a[208:223]\n"
    "global_load_dwordx4 a[184:187], v4, s[36:37] offset:-2048 sc1\n"
    "v_exp_f32 v196, v196\n"
    "v_exp_f32 v197, v197\n"
    "v_cvt_pk_bf16_f32 v121, v194, v195\n"
    "v_exp_f32 v198, v198\n"
    "v_exp_f32 v199, v199\n"
    "v_cvt_pk_bf16_f32 v122, v196, v197\n"
    "v_mfma_f32_32x32x16_bf16 v[176:191], a[116:119], a[172:175], v[176:191]\n"
    "s_mul_i32 s84, s75, 112\n"
    "v_exp_f32 v200, v200\n"
    "v_exp_f32 v201, v201\n"
    "v_cvt_pk_bf16_f32 v123, v198, v199\n"
    "v_exp_f32 v202, v202\n"
    "v_exp_f32 v203, v203\n"
    "v_cvt_pk_bf16_f32 v124, v200, v201\n"
    "v_mfma_f32_32x32x16_bf16 a[224:239], a[120:123], v[120:123], 0\n"
    "s_waitcnt vmcnt(8)\n"
    "v_exp_f32 v204, v204\n"
    "v_exp_f32 v205, v205\n"
    "v_cvt_pk_bf16_f32 v125, v202, v203\n"
    "v_exp_f32 v206, v206\n"
    "v_exp_f32 v207, v207\n"
    "v_cvt_pk_bf16_f32 v126, v204, v205\n"
    "v_cvt_pk_bf16_f32 v127, v206, v207\n"
    "v_mfma_f32_32x32x16_bf16 v[192:207], a[112:115], a[144:147], 0\n"
    "global_load_dwordx4 a[188:191], v4, s[36:37] offset:-1024 sc1\n"
    "v_exp_f32 v176, v176\n"
    "v_exp_f32 v177, v177\n"
    "v_exp_f32 v178, v178\n"
    "v_exp_f32 v179, v179\n"
    "v_cvt_pk_bf16_f32 v120, v176, v177\n"
    "v_mfma_f32_32x32x16_bf16 a[224:239], a[124:127], v[124:127], a[224:239]\n"
    "global_load_dwordx4 a[192:195], v4, s[36:37] offset:0 sc1\n"
    "v_exp_f32 v180, v180\n"
    "v_exp_f32 v181, v181\n"
    "v_cvt_pk_bf16_f32 v121, v178, v179\n"
    "v_exp_f32 v182, v182\n"
    "v_exp_f32 v183, v183\n"
    "v_cvt_pk_bf16_f32 v122, v180, v181\n"
    "v_mfma_f32_32x32x16_bf16 v[192:207], a[116:119], a[148:151], v[192:207]\n"
    "s_add_u32 s66, s66, s84\n"
    "v_exp_f32 v184, v184\n"
    "v_exp_f32 v185, v185\n"
    "v_cvt_pk_bf16_f32 v123, v182, v183\n"
    "v_exp_f32 v186, v186\n"
    "v_exp_f32 v187, v187\n"
    "v_cvt_pk_bf16_f32 v124, v184, v185\n"
    "v_mfma_f32_32x32x16_bf16 a[240:255], a[120:123], v[120:123], 0\n"
    "s_waitcnt vmcnt(6)\n"
    "v_exp_f32 v188, v188\n"
    "v_exp_f32 v189, v189\n"
    "v_cvt_pk_bf16_f32 v125, v186, v187\n"
    "v_exp_f32 v190, v190\n"
    "v_exp_f32 v191, v191\n"
    "v_cvt_pk_bf16_f32 v126, v188, v189\n"
    "v_cvt_pk_bf16_f32 v127, v190, v191\n"
    "v_mfma_f32_32x32x16_bf16 v[176:191], a[128:131], a[152:155], 0\n"
    "global_load_dwordx4 a[196:199], v4, s[36:37] offset:1024 sc1\n"
    "v_exp_f32 v192, v192\n"
    "v_exp_f32 v193, v193\n"
    "v_exp_f32 v194, v194\n"
    "v_exp_f32 v195, v195\n"
    "v_cvt_pk_bf16_f32 v120, v192, v193\n"
    "v_mfma_f32_32x32x16_bf16 a[240:255], a[124:127], v[124:127], a[240:255]\n"
    "s_mul_i32 s67, s72, 14336\n"
    "v_exp_f32 v196, v196\n"
    "v_exp_f32 v197, v197\n"
    "v_cvt_pk_bf16_f32 v121, v194, v195\n"
    "v_exp_f32 v198, v198\n"
    "v_exp_f32 v199, v199\n"
    "v_cvt_pk_bf16_f32 v122, v196, v197\n"
    "v_mfma_f32_32x32x16_bf16 v[176:191], a[132:135], a[156:159], v[176:191]\n"
    "global_load_dwordx4 a[200:203], v4, s[36:37] offset:2048 sc1\n"
    "v_exp_f32 v200, v200\n"
    "v_exp_f32 v201, v201\n"
    "v_cvt_pk_bf16_f32 v123, v198, v199\n"
    "v_exp_f32 v202, v202\n"
    "v_exp_f32 v203, v203\n"
    "v_cvt_pk_bf16_f32 v124, v200, v201\n"
    "v_mfma_f32_32x32x16_bf16 v[160:175], a[120:123], v[120:123], 0\n"
    "s_add_u32 s67, s67, 106496\n"
    "v_exp_f32 v204, v204\n"
    "v_exp_f32 v205, v205\n"
    "v_cvt_pk_bf16_f32 v125, v202, v203\n"
    "v_exp_f32 v206, v206\n"
    "v_exp_f32 v207, v207\n"
    "v_cvt_pk_bf16_f32 v126, v204, v205\n"
    "v_cvt_pk_bf16_f32 v127, v206, v207\n"
    "v_mfma_f32_32x32x16_bf16 v[192:207], a[128:131], a[160:163], 0\n"
    "global_load_dwordx4 a[204:207], v4, s[36:37] offset:3072 sc1\n"
    "v_exp_f32 v176, v176\n"
    "v_exp_f32 v177, v177\n"
    "v_exp_f32 v178, v178\n"
    "v_exp_f32 v179, v179\n"
    "v_cvt_pk_bf16_f32 v120, v176, v177\n"
    "v_mfma_f32_32x32x16_bf16 v[160:175], a[124:127], v[124:127], v[160:175]\n"
    "s_add_u32 s36, s36, 8192\n"
    "s_addc_u32 s37, s37, 0\n"
    "v_exp_f32 v180, v180\n"
    "v_exp_f32 v181, v181\n"
    "v_cvt_pk_bf16_f32 v121, v178, v179\n"
    "v_exp_f32 v182, v182\n"
    "v_exp_f32 v183, v183\n"
    "v_cvt_pk_bf16_f32 v122, v180, v181\n"
    "v_mfma_f32_32x32x16_bf16 v[192:207], a[132:135], a[164:167], v[192:207]\n"
    "global_load_dwordx4 a[112:115], v4, s[36:37] offset:-4096 sc1\n"
    "v_exp_f32 v184, v184\n"
    "v_exp_f32 v185, v185\n"
    "v_cvt_pk_bf16_f32 v123, v182, v183\n"
    "v_exp_f32 v186, v186\n"
    "v_exp_f32 v187, v187\n"
    "v_cvt_pk_bf16_f32 v124, v184, v185\n"
    "v_mfma_f32_32x32x16_bf16 a[208:223], a[136:139], v[120:123], a[208:223]\n"
    "s_waitcnt vmcnt(9)\n"
    "v_exp_f32 v188, v188\n"
    "v_exp_f32 v189, v189\n"
    "v_cvt_pk_bf16_f32 v125, v186, v187\n"
    "v_exp_f32 v190, v190\n"
    "v_exp_f32 v191, v191\n"
    "v_cvt_pk_bf16_f32 v126, v188, v189\n"
    "v_cvt_pk_bf16_f32 v127, v190, v191\n"
    "v_mfma_f32_32x32x16_bf16 v[176:191], a[128:131], a[168:171], 0\n"
    "global_load_dwordx4 a[116:119], v4, s[36:37] offset:-3072 sc1\n"
    "v_exp_f32 v192, v192\n"
    "v_exp_f32 v193, v193\n"
    "v_exp_f32 v194, v194\n"
    "v_exp_f32 v195, v195\n"
    "v_cvt_pk_bf16_f32 v120, v192, v193\n"
    "v_mfma_f32_32x32x16_bf16 a[208:223], a[140:143], v[124:127], a[208:223]\n"
    "buffer_store_dwordx4 v[30:33], v70, s[48:51], s44 offen sc1\n"
    "v_exp_f32 v196, v196\n"
    "v_exp_f32 v197, v197\n"
    "v_cvt_pk_bf16_f32 v121, v194, v195\n"
    "v_exp_f32 v198, v198\n"
    "v_exp_f32 v199, v199\n"
    "v_cvt_pk_bf16_f32 v122, v196, v197\n"
    "v_mfma_f32_32x32x16_bf16 v[176:191], a[132:135], a[172:175], v[176:191]\n"
    "global_load_dwordx4 a[120:123], v4, s[36:37] offset:-2048 sc1\n"
    "v_exp_f32 v200, v200\n"
    "v_exp_f32 v201, v201\n"
    "v_cvt_pk_bf16_f32 v123, v198, v199\n"
    "v_exp_f32 v202, v202\n"
    "v_exp_f32 v203, v203\n"
    "v_cvt_pk_bf16_f32 v124, v200, v201\n"
    "v_mfma_f32_32x32x16_bf16 a[224:239], a[136:139], v[120:123], a[224:239]\n"
    "buffer_store_dwordx4 v[252:255], v4, s[48:51], s57 offen offset:0 sc1\n"
    "v_exp_f32 v204, v204\n"
    "v_exp_f32 v205, v205\n"
    "v_cvt_pk_bf16_f32 v125, v202, v203\n"
    "v_exp_f32 v206, v206\n"
    "v_exp_f32 v207, v207\n"
    "v_cvt_pk_bf16_f32 v126, v204, v205\n"
    "v_cvt_pk_bf16_f32 v127, v206, v207\n"
    "v_mfma_f32_32x32x16_bf16 v[192:207], a[128:131], a[144:147], 0\n"
    "global_load_dwordx4 a[124:127], v4, s[36:37] offset:-1024 sc1\n"
    "v_exp_f32 v176, v176\n"
    "v_exp_f32 v177, v177\n"
    "v_exp_f32 v178, v178\n"
    "v_exp_f32 v179, v179\n"
    "v_cvt_pk_bf16_f32 v120, v176, v177\n"
    "v_mfma_f32_32x32x16_bf16 a[224:239], a[140:143], v[124:127], a[224:239]\n"
    "buffer_store_dwordx4 v[252:255], v4, s[48:51], s57 offen offset:1024 sc1\n"
    "v_exp_f32 v180, v180\n"
    "v_exp_f32 v181, v181\n"
    "v_cvt_pk_bf16_f32 v121, v178, v179\n"
    "v_exp_f32 v182, v182\n"
    "v_exp_f32 v183, v183\n"
    "v_cvt_pk_bf16_f32 v122, v180, v181\n"
    "v_mfma_f32_32x32x16_bf16 v[192:207], a[132:135], a[148:151], v[192:207]\n"
    "s_add_u32 s32, s32, s62\n"
    "s_addc_u32 s33, s33, 0\n"
    "v_exp_f32 v184, v184\n"
    "v_exp_f32 v185, v185\n"
    "v_cvt_pk_bf16_f32 v123, v182, v183\n"
    "v_exp_f32 v186, v186\n"
    "v_exp_f32 v187, v187\n"
    "v_cvt_pk_bf16_f32 v124, v184, v185\n"
    "v_mfma_f32_32x32x16_bf16 a[240:255], a[136:139], v[120:123], a[240:255]\n"
    "s_waitcnt vmcnt(11)\n"
    "v_exp_f32 v188, v188\n"
    "v_exp_f32 v189, v189\n"
    "v_cvt_pk_bf16_f32 v125, v186, v187\n"
    "v_exp_f32 v190, v190\n"
    "v_exp_f32 v191, v191\n"
    "v_cvt_pk_bf16_f32 v126, v188, v189\n"
    "v_cvt_pk_bf16_f32 v127, v190, v191\n"
    "v_mfma_f32_32x32x16_bf16 v[176:191], a[176:179], a[152:155], 0\n"
    "s_mov_b32 m0, s63\n"
    "v_exp_f32 v192, v192\n"
    "v_exp_f32 v193, v193\n"
    "v_exp_f32 v194, v194\n"
    "v_exp_f32 v195, v195\n"
    "v_cvt_pk_bf16_f32 v120, v192, v193\n"
    "v_mfma_f32_32x32x16_bf16 a[240:255], a[140:143], v[124:127], a[240:255]\n"
    "global_load_lds_dwordx4 v75, s[32:33] offset:0\n"
    "v_exp_f32 v196, v196\n"
    "v_exp_f32 v197, v197\n"
    "v_cvt_pk_bf16_f32 v121, v194, v195\n"
    "v_exp_f32 v198, v198\n"
    "v_exp_f32 v199, v199\n"
    "v_cvt_pk_bf16_f32 v122, v196, v197\n"
    "v_mfma_f32_32x32x16_bf16 v[176:191], a[180:183], a[156:159], v[176:191]\n"
    "s_mul_i32 s47, s64, 14\n"
    "v_exp_f32 v200, v200\n"
    "v_exp_f32 v201, v201\n"
    "v_cvt_pk_bf16_f32 v123, v198, v199\n"
    "v_exp_f32 v202, v202\n"
    "v_exp_f32 v203, v203\n"
    "v_cvt_pk_bf16_f32 v124, v200, v201\n"
    "v_mfma_f32_32x32x16_bf16 v[160:175], a[136:139], v[120:123], v[160:175]\n"
    "global_load_lds_dwordx4 v76, s[32:33] offset:1024\n"
    "v_exp_f32 v204, v204\n"
    "v_exp_f32 v205, v205\n"
    "v_cvt_pk_bf16_f32 v125, v202, v203\n"
    "v_exp_f32 v206, v206\n"
    "v_exp_f32 v207, v207\n"
    "v_cvt_pk_bf16_f32 v126, v204, v205\n"
    "v_cvt_pk_bf16_f32 v127, v206, v207\n"
    "v_mfma_f32_32x32x16_bf16 v[192:207], a[176:179], a[160:163], 0\n"
    "s_add_u32 s47, s47, s74\n"
    "v_exp_f32 v176, v176\n"
    "v_exp_f32 v177, v177\n"
    "v_exp_f32 v178, v178\n"
    "v_exp_f32 v179, v179\n"
    "v_cvt_pk_bf16_f32 v120, v176, v177\n"
    "v_mfma_f32_32x32x16_bf16 v[160:175], a[140:143], v[124:127], v[160:175]\n"
    "global_load_lds_dwordx4 v77, s[32:33] offset:2048\n"
    "v_exp_f32 v180, v180\n"
    "v_exp_f32 v181, v181\n"
    "v_cvt_pk_bf16_f32 v121, v178, v179\n"
    "v_exp_f32 v182, v182\n"
    "v_exp_f32 v183, v183\n"
    "v_cvt_pk_bf16_f32 v122, v180, v181\n"
    "v_mfma_f32_32x32x16_bf16 v[192:207], a[180:183], a[164:167], v[192:207]\n"
    "global_load_dwordx4 a[128:131], v4, s[36:37] offset:0 sc1\n"
    "v_exp_f32 v184, v184\n"
    "v_exp_f32 v185, v185\n"
    "v_cvt_pk_bf16_f32 v123, v182, v183\n"
    "v_exp_f32 v186, v186\n"
    "v_exp_f32 v187, v187\n"
    "v_cvt_pk_bf16_f32 v124, v184, v185\n"
    "v_mfma_f32_32x32x16_bf16 a[208:223], a[184:187], v[120:123], a[208:223]\n"
    "global_load_lds_dwordx4 v78, s[32:33] offset:3072\n"
    "v_exp_f32 v188, v188\n"
    "v_exp_f32 v189, v189\n"
    "v_cvt_pk_bf16_f32 v125, v186, v187\n"
    "v_exp_f32 v190, v190\n"
    "v_exp_f32 v191, v191\n"
    "v_cvt_pk_bf16_f32 v126, v188, v189\n"
    "v_cvt_pk_bf16_f32 v127, v190, v191\n"
    "v_mfma_f32_32x32x16_bf16 v[176:191], a[176:179], a[168:171], 0\n"
    "s_lshl_b32 s47, s47, 12\n"
    "v_exp_f32 v192, v192\n"
    "v_exp_f32 v193, v193\n"
    "v_exp_f32 v194, v194\n"
    "v_exp_f32 v195, v195\n"
    "v_cvt_pk_bf16_f32 v120, v192, v193\n"
    "v_mfma_f32_32x32x16_bf16 a[208:223], a[188:191], v[124:127], a[208:223]\n"
    "s_add_u32 m0, m0, 4096\n"
    "v_exp_f32 v196, v196\n"
    "v_exp_f32 v197, v197\n"
    "v_cvt_pk_bf16_f32 v121, v194, v195\n"
    "v_exp_f32 v198, v198\n"
    "v_exp_f32 v199, v199\n"
    "v_cvt_pk_bf16_f32 v122, v196, v197\n"
    "v_mfma_f32_32x32x16_bf16 v[176:191], a[180:183], a[172:175], v[176:191]\n"
    "global_load_dwordx4 a[132:135], v4, s[36:37] offset:1024 sc1\n"
    "v_exp_f32 v200, v200\n"
    "v_exp_f32 v201, v201\n"
    "v_cvt_pk_bf16_f32 v123, v198, v199\n"
    "v_exp_f32 v202, v202\n"
    "v_exp_f32 v203, v203\n"
    "v_cvt_pk_bf16_f32 v124, v200, v201\n"
    "v_mfma_f32_32x32x16_bf16 a[224:239], a[184:187], v[120:123], a[224:239]\n"
    "global_load_lds_dwordx4 v79, s[32:33] offset:0\n"
    "v_exp_f32 v204, v204\n"
    "v_exp_f32 v205, v205\n"
    "v_cvt_pk_bf16_f32 v125, v202, v203\n"
    "v_exp_f32 v206, v206\n"
    "v_exp_f32 v207, v207\n"
    "v_cvt_pk_bf16_f32 v126, v204, v205\n"
    "v_cvt_pk_bf16_f32 v127, v206, v207\n"
    "v_mfma_f32_32x32x16_bf16 v[192:207], a[176:179], a[144:147], 0\n"
    "s_add_u32 s47, s47, 524288\n"
    "v_exp_f32 v176, v176\n"
    "v_exp_f32 v177, v177\n"
    "v_exp_f32 v178, v178\n"
    "v_exp_f32 v179, v179\n"
    "v_cvt_pk_bf16_f32 v120, v176, v177\n"
    "v_mfma_f32_32x32x16_bf16 a[224:239], a[188:191], v[124:127], a[224:239]\n"
    "global_load_lds_dwordx4 v80, s[32:33] offset:1024\n"
    "v_exp_f32 v180, v180\n"
    "v_exp_f32 v181, v181\n"
    "v_cvt_pk_bf16_f32 v121, v178, v179\n"
    "v_exp_f32 v182, v182\n"
    "v_exp_f32 v183, v183\n"
    "v_cvt_pk_bf16_f32 v122, v180, v181\n"
    "v_mfma_f32_32x32x16_bf16 v[192:207], a[180:183], a[148:151], v[192:207]\n"
    "global_load_dwordx4 a[136:139], v4, s[36:37] offset:2048 sc1\n"
    "v_exp_f32 v184, v184\n"
    "v_exp_f32 v185, v185\n"
    "v_cvt_pk_bf16_f32 v123, v182, v183\n"
    "v_exp_f32 v186, v186\n"
    "v_exp_f32 v187, v187\n"
    "v_cvt_pk_bf16_f32 v124, v184, v185\n"
    "v_mfma_f32_32x32x16_bf16 a[240:255], a[184:187], v[120:123], a[240:255]\n"
    "s_waitcnt vmcnt(16)\n"
    "v_exp_f32 v188, v188\n"
    "v_exp_f32 v189, v189\n"
    "v_cvt_pk_bf16_f32 v125, v186, v187\n"
    "v_exp_f32 v190, v190\n"
    "v_exp_f32 v191, v191\n"
    "v_cvt_pk_bf16_f32 v126, v188, v189\n"
    "v_cvt_pk_bf16_f32 v127, v190, v191\n"
    "v_mfma_f32_32x32x16_bf16 v[176:191], a[192:195], a[152:155], 0\n"
    "buffer_load_dwordx2 v[60:61], v95, s[48:51], s39 offen sc1\n"
    "v_exp_f32 v192, v192\n"
    "v_exp_f32 v193, v193\n"
    "v_exp_f32 v194, v194\n"
    "v_exp_f32 v195, v195\n"
    "v_cvt_pk_bf16_f32 v120, v192, v193\n"
    "v_mfma_f32_32x32x16_bf16 a[240:255], a[188:191], v[124:127], a[240:255]\n"
    "s_bfm_b64 s[80:81], s76, 0\n"
    "v_exp_f32 v196, v196\n"
    "v_exp_f32 v197, v197\n"
    "v_cvt_pk_bf16_f32 v121, v194, v195\n"
    "v_exp_f32 v198, v198\n"
    "v_exp_f32 v199, v199\n"
    "v_cvt_pk_bf16_f32 v122, v196, v197\n"
    "v_mfma_f32_32x32x16_bf16 v[176:191], a[196:199], a[156:159], v[176:191]\n"
    "global_load_dwordx4 a[140:143], v4, s[36:37] offset:3072 sc1\n"
    "v_exp_f32 v200, v200\n"
    "v_exp_f32 v201, v201\n"
    "v_cvt_pk_bf16_f32 v123, v198, v199\n"
    "v_exp_f32 v202, v202\n"
    "v_exp_f32 v203, v203\n"
    "v_cvt_pk_bf16_f32 v124, v200, v201\n"
    "v_mfma_f32_32x32x16_bf16 v[160:175], a[184:187], v[120:123], v[160:175]\n"
    "global_load_lds_dwordx4 v81, s[32:33] offset:2048\n"
    "v_exp_f32 v204, v204\n"
    "v_exp_f32 v205, v205\n"
    "v_cvt_pk_bf16_f32 v125, v202, v203\n"
    "v_exp_f32 v206, v206\n"
    "v_exp_f32 v207, v207\n"
    "v_cvt_pk_bf16_f32 v126, v204, v205\n"
    "v_cvt_pk_bf16_f32 v127, v206, v207\n"
    "v_mfma_f32_32x32x16_bf16 v[192:207], a[192:195], a[160:163], 0\n"
    "s_mul_i32 s45, s75, 7168\n"
    "v_exp_f32 v176, v176\n"
    "v_exp_f32 v177, v177\n"
    "v_exp_f32 v178, v178\n"
    "v_exp_f32 v179, v179\n"
    "v_cvt_pk_bf16_f32 v120, v176, v177\n"
    "v_mfma_f32_32x32x16_bf16 v[160:175], a[188:191], v[124:127], v[160:175]\n"
    "s_add_u32 s36, s36, 8192\n"
    "s_addc_u32 s37, s37, 0\n"
    "v_exp_f32 v180, v180\n"
    "v_exp_f32 v181, v181\n"
    "v_cvt_pk_bf16_f32 v121, v178, v179\n"
    "v_exp_f32 v182, v182\n"
    "v_exp_f32 v183, v183\n"
    "v_cvt_pk_bf16_f32 v122, v180, v181\n"
    "v_mfma_f32_32x32x16_bf16 v[192:207], a[196:199], a[164:167], v[192:207]\n"
    "global_load_dwordx4 a[176:179], v4, s[36:37] offset:-4096 sc1\n"
    "v_exp_f32 v184, v184\n"
    "v_exp_f32 v185, v185\n"
    "v_cvt_pk_bf16_f32 v123, v182, v183\n"
    "v_exp_f32 v186, v186\n"
    "v_exp_f32 v187, v187\n"
    "v_cvt_pk_bf16_f32 v124, v184, v185\n"
    "v_mfma_f32_32x32x16_bf16 a[208:223], a[200:203], v[120:123], a[208:223]\n"
    "s_add_u32 s32, s32, 256\n"
    "s_addc_u32 s33, s33, 0\n"
    "v_exp_f32 v188, v188\n"
    "v_exp_f32 v189, v189\n"
    "v_cvt_pk_bf16_f32 v125, v186, v187\n"
    "v_exp_f32 v190, v190\n"
    "v_exp_f32 v191, v191\n"
    "v_cvt_pk_bf16_f32 v126, v188, v189\n"
    "v_cvt_pk_bf16_f32 v127, v190, v191\n"
    "v_mfma_f32_32x32x16_bf16 v[176:191], a[192:195], a[168:171], 0\n"
    "s_add_u32 s63, s63, 28672\n"
    "v_exp_f32 v192, v192\n"
    "v_exp_f32 v193, v193\n"
    "v_exp_f32 v194, v194\n"
    "v_exp_f32 v195, v195\n"
    "v_cvt_pk_bf16_f32 v120, v192, v193\n"
    "v_mfma_f32_32x32x16_bf16 a[208:223], a[204:207], v[124:127], a[208:223]\n"
    "s_mov_b32 m0, s63\n"
    "v_exp_f32 v196, v196\n"
    "v_exp_f32 v197, v197\n"
    "v_cvt_pk_bf16_f32 v121, v194, v195\n"
    "v_exp_f32 v198, v198\n"
    "v_exp_f32 v199, v199\n"
    "v_cvt_pk_bf16_f32 v122, v196, v197\n"
    "v_mfma_f32_32x32x16_bf16 v[176:191], a[196:199], a[172:175], v[176:191]\n"
    "global_load_dwordx4 a[180:183], v4, s[36:37] offset:-3072 sc1\n"
    "v_exp_f32 v200, v200\n"
    "v_exp_f32 v201, v201\n"
    "v_cvt_pk_bf16_f32 v123, v198, v199\n"
    "v_exp_f32 v202, v202\n"
    "v_exp_f32 v203, v203\n"
    "v_cvt_pk_bf16_f32 v124, v200, v201\n"
    "v_mfma_f32_32x32x16_bf16 a[224:239], a[200:203], v[120:123], a[224:239]\n"
    "global_load_lds_dwordx4 v75, s[32:33] offset:0\n"
    "v_exp_f32 v204, v204\n"
    "v_exp_f32 v205, v205\n"
    "v_cvt_pk_bf16_f32 v125, v202, v203\n"
    "v_exp_f32 v206, v206\n"
    "v_exp_f32 v207, v207\n"
    "v_cvt_pk_bf16_f32 v126, v204, v205\n"
    "v_cvt_pk_bf16_f32 v127, v206, v207\n"
    "v_mfma_f32_32x32x16_bf16 v[192:207], a[192:195], a[144:147], 0\n"
    "s_waitcnt vmcnt(5)\n"
    "v_cmp_ne_u64 vcc, s[78:79], v[60:61]\n"
    "v_exp_f32 v176, v176\n"
    "v_exp_f32 v177, v177\n"
    "v_exp_f32 v178, v178\n"
    "v_exp_f32 v179, v179\n"
    "v_cvt_pk_bf16_f32 v120, v176, v177\n"
    "v_mfma_f32_32x32x16_bf16 a[224:239], a[204:207], v[124:127], a[224:239]\n"
    "s_and_b64 vcc, vcc, s[80:81]\n"
    "s_cbranch_scc1 Lfesl_%=\n"
    "Lfeok_%=:\n"
    "v_exp_f32 v180, v180\n"
    "v_exp_f32 v181, v181\n"
    "v_cvt_pk_bf16_f32 v121, v178, v179\n"
    "v_exp_f32 v182, v182\n"
    "v_exp_f32 v183, v183\n"
    "v_cvt_pk_bf16_f32 v122, v180, v181\n"
    "v_mfma_f32_32x32x16_bf16 v[192:207], a[196:199], a[148:151], v[192:207]\n"
    "global_load_dwordx4 a[184:187], v4, s[36:37] offset:-2048 sc1\n"
    "v_exp_f32 v184, v184\n"
    "v_exp_f32 v185, v185\n"
    "v_cvt_pk_bf16_f32 v123, v182, v183\n"
    "v_exp_f32 v186, v186\n"
    "v_exp_f32 v187, v187\n"
    "v_cvt_pk_bf16_f32 v124, v184, v185\n"
    "v_mfma_f32_32x32x16_bf16 a[240:255], a[200:203], v[120:123], a[240:255]\n"
    "s_waitcnt vmcnt(17)\n"
    "v_exp_f32 v188, v188\n"
    "v_exp_f32 v189, v189\n"
    "v_cvt_pk_bf16_f32 v125, v186, v187\n"
    "v_exp_f32 v190, v190\n"
    "v_exp_f32 v191, v191\n"
    "v_cvt_pk_bf16_f32 v126, v188, v189\n"
    "v_cvt_pk_bf16_f32 v127, v190, v191\n"
    "v_mfma_f32_32x32x16_bf16 v[176:191], a[112:115], a[152:155], 0\n"
    "buffer_load_dwordx2 v[238:239], v62, s[20:23], 0 offen offset:3584 sc1\n"
    "v_exp_f32 v192, v192\n"
    "v_exp_f32 v193, v193\n"
    "v_exp_f32 v194, v194\n"
    "v_exp_f32 v195, v195\n"
    "v_cvt_pk_bf16_f32 v120, v192, v193\n"
    "v_mfma_f32_32x32x16_bf16 a[240:255], a[204:207], v[124:127], a[240:255]\n"
    "buffer_load_dwordx2 v[236:237], v62, s[20:23], 0 offen offset:3072 sc1\n"
    "v_exp_f32 v196, v196\n"
    "v_exp_f32 v197, v197\n"
    "v_cvt_pk_bf16_f32 v121, v194, v195\n"
    "v_exp_f32 v198, v198\n"
    "v_exp_f32 v199, v199\n"
    "v_cvt_pk_bf16_f32 v122, v196, v197\n"
    "v_mfma_f32_32x32x16_bf16 v[176:191], a[116:119], a[156:159], v[176:191]\n"
    "global_load_dwordx4 a[188:191], v4, s[36:37] offset:-1024 sc1\n"
    "v_exp_f32 v200, v200\n"
    "v_exp_f32 v201, v201\n"
    "v_cvt_pk_bf16_f32 v123, v198, v199\n"
    "v_exp_f32 v202, v202\n"
    "v_exp_f32 v203, v203\n"
    "v_cvt_pk_bf16_f32 v124, v200, v201\n"
    "v_mfma_f32_32x32x16_bf16 v[160:175], a[200:203], v[120:123], v[160:175]\n"
    "buffer_load_dwordx2 v[234:235], v62, s[20:23], 0 offen offset:2560 sc1\n"
    "v_exp_f32 v204, v204\n"
    "v_exp_f32 v205, v205\n"
    "v_cvt_pk_bf16_f32 v125, v202, v203\n"
    "v_exp_f32 v206, v206\n"
    "v_exp_f32 v207, v207\n"
    "v_cvt_pk_bf16_f32 v126, v204, v205\n"
    "v_cvt_pk_bf16_f32 v127, v206, v207\n"
    "v_mfma_f32_32x32x16_bf16 v[192:207], a[112:115], a[160:163], 0\n"
    "buffer_load_dwordx2 v[232:233], v62, s[20:23], 0 offen offset:2048 sc1\n"
    "v_exp_f32 v176, v176\n"
    "v_exp_f32 v177, v177\n"
    "v_exp_f32 v178, v178\n"
    "v_exp_f32 v179, v179\n"
    "v_cvt_pk_bf16_f32 v120, v176, v177\n"
    "v_mfma_f32_32x32x16_bf16 v[160:175], a[204:207], v[124:127], v[160:175]\n"
    "buffer_load_dwordx2 v[230:231], v62, s[20:23], 0 offen offset:1536 sc1\n"
    "v_exp_f32 v180, v180\n"
    "v_exp_f32 v181, v181\n"
    "v_cvt_pk_bf16_f32 v121, v178, v179\n"
    "v_exp_f32 v182, v182\n"
    "v_exp_f32 v183, v183\n"
    "v_cvt_pk_bf16_f32 v122, v180, v181\n"
    "v_mfma_f32_32x32x16_bf16 v[192:207], a[116:119], a[164:167], v[192:207]\n"
    "global_load_dwordx4 a[192:195], v4, s[36:37] offset:0 sc1\n"
    "v_exp_f32 v184, v184\n"
    "v_exp_f32 v185, v185\n"
    "v_cvt_pk_bf16_f32 v123, v182, v183\n"
    "v_exp_f32 v186, v186\n"
    "v_exp_f32 v187, v187\n"
    "v_cvt_pk_bf16_f32 v124, v184, v185\n"
    "v_mfma_f32_32x32x16_bf16 a[208:223], a[120:123], v[120:123], a[208:223]\n"
    "buffer_load_dwordx2 v[228:229], v62, s[20:23], 0 offen offset:1024 sc1\n"
    "v_exp_f32 v188, v188\n"
    "v_exp_f32 v189, v189\n"
    "v_cvt_pk_bf16_f32 v125, v186, v187\n"
    "v_exp_f32 v190, v190\n"
    "v_exp_f32 v191, v191\n"
    "v_cvt_pk_bf16_f32 v126, v188, v189\n"
    "v_cvt_pk_bf16_f32 v127, v190, v191\n"
    "v_mfma_f32_32x32x16_bf16 v[176:191], a[112:115], a[168:171], 0\n"
    "buffer_load_dwordx2 v[226:227], v62, s[20:23], 0 offen offset:512 sc1\n"
    "v_exp_f32 v192, v192\n"
    "v_exp_f32 v193, v193\n"
    "v_exp_f32 v194, v194\n"
    "v_exp_f32 v195, v195\n"
    "v_cvt_pk_bf16_f32 v120, v192, v193\n"
    "v_mfma_f32_32x32x16_bf16 a[208:223], a[124:127], v[124:127], a[208:223]\n"
    "buffer_load_dwordx2 v[224:225], v62, s[20:23], 0 offen offset:0 sc1\n"
    "v_exp_f32 v196, v196\n"
    "v_exp_f32 v197, v197\n"
    "v_cvt_pk_bf16_f32 v121, v194, v195\n"
    "v_exp_f32 v198, v198\n"
    "v_exp_f32 v199, v199\n"
    "v_cvt_pk_bf16_f32 v122, v196, v197\n"
    "v_mfma_f32_32x32x16_bf16 v[176:191], a[116:119], a[172:175], v[176:191]\n"
    "global_load_dwordx4 a[196:199], v4, s[36:37] offset:1024 sc1\n"
    "v_exp_f32 v200, v200\n"
    "v_exp_f32 v201, v201\n"
    "v_cvt_pk_bf16_f32 v123, v198, v199\n"
    "v_exp_f32 v202, v202\n"
    "v_exp_f32 v203, v203\n"
    "v_cvt_pk_bf16_f32 v124, v200, v201\n"
    "v_mfma_f32_32x32x16_bf16 a[224:239], a[120:123], v[120:123], a[224:239]\n"
    "buffer_load_dwordx2 v[222:223], v59, s[20:23], 0 offen offset:3584 sc1\n"
    "v_exp_f32 v204, v204\n"
    "v_exp_f32 v205, v205\n"
    "v_cvt_pk_bf16_f32 v125, v202, v203\n"
    "v_exp_f32 v206, v206\n"
    "v_exp_f32 v207, v207\n"
    "v_cvt_pk_bf16_f32 v126, v204, v205\n"
    "v_cvt_pk_bf16_f32 v127, v206, v207\n"
    "v_mfma_f32_32x32x16_bf16 v[192:207], a[112:115], a[144:147], 0\n"
    "buffer_load_dwordx2 v[220:221], v59, s[20:23], 0 offen offset:3072 sc1\n"
    "v_exp_f32 v176, v176\n"
    "v_exp_f32 v177, v177\n"
    "v_exp_f32 v178, v178\n"
    "v_exp_f32 v179, v179\n"
    "v_cvt_pk_bf16_f32 v120, v176, v177\n"
    "v_mfma_f32_32x32x16_bf16 a[224:239], a[124:127], v[124:127], a[224:239]\n"
    "buffer_load_dwordx2 v[218:219], v59, s[20:23], 0 offen offset:2560 sc1\n"
    "v_exp_f32 v180, v180\n"
    "v_exp_f32 v181, v181\n"
    "v_cvt_pk_bf16_f32 v121, v178, v179\n"
    "v_exp_f32 v182, v182\n"
    "v_exp_f32 v183, v183\n"
    "v_cvt_pk_bf16_f32 v122, v180, v181\n"
    "v_mfma_f32_32x32x16_bf16 v[192:207], a[116:119], a[148:151], v[192:207]\n"
    "global_load_dwordx4 a[200:203], v4, s[36:37] offset:2048 sc1\n"
    "v_exp_f32 v184, v184\n"
    "v_exp_f32 v185, v185\n"
    "v_cvt_pk_bf16_f32 v123, v182, v183\n"
    "v_exp_f32 v186, v186\n"
    "v_exp_f32 v187, v187\n"
    "v_cvt_pk_bf16_f32 v124, v184, v185\n"
    "v_mfma_f32_32x32x16_bf16 a[240:255], a[120:123], v[120:123], a[240:255]\n"
    "s_waitcnt vmcnt(20)\n"
    "v_exp_f32 v188, v188\n"
    "v_exp_f32 v189, v189\n"
    "v_cvt_pk_bf16_f32 v125, v186, v187\n"
    "v_exp_f32 v190, v190\n"
    "v_exp_f32 v191, v191\n"
    "v_cvt_pk_bf16_f32 v126, v188, v189\n"
    "v_cvt_pk_bf16_f32 v127, v190, v191\n"
    "v_mfma_f32_32x32x16_bf16 v[176:191], a[128:131], a[152:155], 0\n"
    "buffer_load_dwordx2 v[216:217], v59, s[20:23], 0 offen offset:2048 sc1\n"
    "v_exp_f32 v192, v192\n"
    "v_exp_f32 v193, v193\n"
    "v_exp_f32 v194, v194\n"
    "v_exp_f32 v195, v195\n"
    "v_cvt_pk_bf16_f32 v120, v192, v193\n"
    "v_mfma_f32_32x32x16_bf16 a[240:255], a[124:127], v[124:127], a[240:255]\n"
    "buffer_load_dwordx2 v[214:215], v59, s[20:23], 0 offen offset:1536 sc1\n"
    "v_exp_f32 v196, v196\n"
    "v_exp_f32 v197, v197\n"
    "v_cvt_pk_bf16_f32 v121, v194, v195\n"
    "v_exp_f32 v198, v198\n"
    "v_exp_f32 v199, v199\n"
    "v_cvt_pk_bf16_f32 v122, v196, v197\n"
    "v_mfma_f32_32x32x16_bf16 v[176:191], a[132:135], a[156:159], v[176:191]\n"
    "global_load_dwordx4 a[204:207], v4, s[36:37] offset:3072 sc1\n"
    "v_exp_f32 v200, v200\n"
    "v_exp_f32 v201, v201\n"
    "v_cvt_pk_bf16_f32 v123, v198, v199\n"
    "v_exp_f32 v202, v202\n"
    "v_exp_f32 v203, v203\n"
    "v_cvt_pk_bf16_f32 v124, v200, v201\n"
    "v_mfma_f32_32x32x16_bf16 v[160:175], a[120:123], v[120:123], v[160:175]\n"
    "buffer_load_dwordx2 v[212:213], v59, s[20:23], 0 offen offset:1024 sc1\n"
    "v_exp_f32 v204, v204\n"
    "v_exp_f32 v205, v205\n"
    "v_cvt_pk_bf16_f32 v125, v202, v203\n"
    "v_exp_f32 v206, v206\n"
    "v_exp_f32 v207, v207\n"
    "v_cvt_pk_bf16_f32 v126, v204, v205\n"
    "v_cvt_pk_bf16_f32 v127, v206, v207\n"
    "v_mfma_f32_32x32x16_bf16 v[192:207], a[128:131], a[160:163], 0\n"
    "buffer_load_dwordx2 v[210:211], v59, s[20:23], 0 offen offset:512 sc1\n"
    "v_exp_f32 v176, v176\n"
    "v_exp_f32 v177, v177\n"
    "v_exp_f32 v178, v178\n"
    "v_exp_f32 v179, v179\n"
    "v_cvt_pk_bf16_f32 v120, v176, v177\n"
    "v_mfma_f32_32x32x16_bf16 v[160:175], a[124:127], v[124:127], v[160:175]\n"
    "s_add_u32 s36, s36, 8192\n"
    "s_addc_u32 s37, s37, 0\n"
    "v_exp_f32 v180, v180\n"
    "v_exp_f32 v181, v181\n"
    "v_cvt_pk_bf16_f32 v121, v178, v179\n"
    "v_exp_f32 v182, v182\n"
    "v_exp_f32 v183, v183\n"
    "v_cvt_pk_bf16_f32 v122, v180, v181\n"
    "v_mfma_f32_32x32x16_bf16 v[192:207], a[132:135], a[164:167], v[192:207]\n"
    "global_load_dwordx4 a[112:115], v4, s[36:37] offset:-4096 sc1\n"
    "v_exp_f32 v184, v184\n"
    "v_exp_f32 v185, v185\n"
    "v_cvt_pk_bf16_f32 v123, v182, v183\n"
    "v_exp_f32 v186, v186\n"
    "v_exp_f32 v187, v187\n"
    "v_cvt_pk_bf16_f32 v124, v184, v185\n"
    "v_mfma_f32_32x32x16_bf16 a[208:223], a[136:139], v[120:123], a[208:223]\n"
    "buffer_load_dwordx2 v[208:209], v59, s[20:23], 0 offen offset:0 sc1\n"
    "v_exp_f32 v188, v188\n"
    "v_exp_f32 v189, v189\n"
    "v_cvt_pk_bf16_f32 v125, v186, v187\n"
    "v_exp_f32 v190, v190\n"
    "v_exp_f32 v191, v191\n"
    "v_cvt_pk_bf16_f32 v126, v188, v189\n"
    "v_cvt_pk_bf16_f32 v127, v190, v191\n"
    "v_mfma_f32_32x32x16_bf16 v[176:191], a[128:131], a[168:171], 0\n"
    "global_load_lds_dwordx4 v76, s[32:33] offset:1024\n"
    "v_exp_f32 v192, v192\n"
    "v_exp_f32 v193, v193\n"
    "v_exp_f32 v194, v194\n"
    "v_exp_f32 v195, v195\n"
    "v_cvt_pk_bf16_f32 v120, v192, v193\n"
    "v_mfma_f32_32x32x16_bf16 a[208:223], a[140:143], v[124:127], a[208:223]\n"
    "s_add_u32 s45, s47, s45\n"
    "v_exp_f32 v196, v196\n"
    "v_exp_f32 v197, v197\n"
    "v_cvt_pk_bf16_f32 v121, v194, v195\n"
    "v_exp_f32 v198, v198\n"
    "v_exp_f32 v199, v199\n"
    "v_cvt_pk_bf16_f32 v122, v196, v197\n"
    "v_mfma_f32_32x32x16_bf16 v[176:191], a[132:135], a[172:175], v[176:191]\n"
    "global_load_dwordx4 a[116:119], v4, s[36:37] offset:-3072 sc1\n"
    "v_exp_f32 v200, v200\n"
    "v_exp_f32 v201, v201\n"
    "v_cvt_pk_bf16_f32 v123, v198, v199\n"
    "v_exp_f32 v202, v202\n"
    "v_exp_f32 v203, v203\n"
    "v_cvt_pk_bf16_f32 v124, v200, v201\n"
    "v_mfma_f32_32x32x16_bf16 a[224:239], a[136:139], v[120:123], a[224:239]\n"
    "global_load_lds_dwordx4 v77, s[32:33] offset:2048\n"
    "v_exp_f32 v204, v204\n"
    "v_exp_f32 v205, v205\n"
    "v_cvt_pk_bf16_f32 v125, v202, v203\n"
    "v_exp_f32 v206, v206\n"
    "v_exp_f32 v207, v207\n"
    "v_cvt_pk_bf16_f32 v126, v204, v205\n"
    "v_cvt_pk_bf16_f32 v127, v206, v207\n"
    "v_mfma_f32_32x32x16_bf16 v[192:207], a[128:131], a[144:147], 0\n"
    "s_lshl_b32 s58, s58, 7\n"
    "v_exp_f32 v176, v176\n"
    "v_exp_f32 v177, v177\n"
    "v_exp_f32 v178, v178\n"
    "v_exp_f32 v179, v179\n"
    "v_cvt_pk_bf16_f32 v120, v176, v177\n"
    "v_mfma_f32_32x32x16_bf16 a[224:239], a[140:143], v[124:127], a[224:239]\n"
    "global_load_lds_dwordx4 v78, s[32:33] offset:3072\n"
    "v_exp_f32 v180, v180\n"
    "v_exp_f32 v181, v181\n"
    "v_cvt_pk_bf16_f32 v121, v178, v179\n"
    "v_exp_f32 v182, v182\n"
    "v_exp_f32 v183, v183\n"
    "v_cvt_pk_bf16_f32 v122, v180, v181\n"
    "v_mfma_f32_32x32x16_bf16 v[192:207], a[132:135], a[148:151], v[192:207]\n"
    "global_load_dwordx4 a[120:123], v4, s[36:37] offset:-2048 sc1\n"
    "v_exp_f32 v184, v184\n"
    "v_exp_f32 v185, v185\n"
    "v_cvt_pk_bf16_f32 v123, v182, v183\n"
    "v_exp_f32 v186, v186\n"
    "v_exp_f32 v187, v187\n"
    "v_cvt_pk_bf16_f32 v124, v184, v185\n"
    "v_mfma_f32_32x32x16_bf16 a[240:255], a[136:139], v[120:123], a[240:255]\n"
    "s_waitcnt vmcnt(24)\n"
    "v_exp_f32 v188, v188\n"
    "v_exp_f32 v189, v189\n"
    "v_cvt_pk_bf16_f32 v125, v186, v187\n"
    "v_exp_f32 v190, v190\n"
    "v_exp_f32 v191, v191\n"
    "v_cvt_pk_bf16_f32 v126, v188, v189\n"
    "v_cvt_pk_bf16_f32 v127, v190, v191\n"
    "v_mfma_f32_32x32x16_bf16 v[176:191], a[176:179], a[152:155], 0\n"
    "s_add_u32 m0, m0, 4096\n"
    "v_exp_f32 v192, v192\n"
    "v_exp_f32 v193, v193\n"
    "v_exp_f32 v194, v194\n"
    "v_exp_f32 v195, v195\n"
    "v_cvt_pk_bf16_f32 v120, v192, v193\n"
    "v_mfma_f32_32x32x16_bf16 a[240:255], a[140:143], v[124:127], a[240:255]\n"
    "global_load_lds_dwordx4 v79, s[32:33] offset:0\n"
    "v_exp_f32 v196, v196\n"
    "v_exp_f32 v197, v197\n"
    "v_cvt_pk_bf16_f32 v121, v194, v195\n"
    "v_exp_f32 v198, v198\n"
    "v_exp_f32 v199, v199\n"
    "v_cvt_pk_bf16_f32 v122, v196, v197\n"
    "v_mfma_f32_32x32x16_bf16 v[176:191], a[180:183], a[156:159], v[176:191]\n"
    "global_load_dwordx4 a[124:127], v4, s[36:37] offset:-1024 sc1\n"
    "v_exp_f32 v200, v200\n"
    "v_exp_f32 v201, v201\n"
    "v_cvt_pk_bf16_f32 v123, v198, v199\n"
    "v_exp_f32 v202, v202\n"
    "v_exp_f32 v203, v203\n"
    "v_cvt_pk_bf16_f32 v124, v200, v201\n"
    "v_mfma_f32_32x32x16_bf16 v[160:175], a[136:139], v[120:123], v[160:175]\n"
    "global_load_lds_dwordx4 v80, s[32:33] offset:1024\n"
    "v_exp_f32 v204, v204\n"
    "v_exp_f32 v205, v205\n"
    "v_cvt_pk_bf16_f32 v125, v202, v203\n"
    "v_exp_f32 v206, v206\n"
    "v_exp_f32 v207, v207\n"
    "v_cvt_pk_bf16_f32 v126, v204, v205\n"
    "v_cvt_pk_bf16_f32 v127, v206, v207\n"
    "s_waitcnt vmcnt(8)\n"
    "v_pk_fma_f32 v[116:117], v[108:109], v[238:239], v[236:237] op_sel_hi:[0,1,1]\n"
    "v_pk_fma_f32 v[18:19], v[108:109], v[222:223], v[220:221] op_sel_hi:[0,1,1]\n"
    "v_pk_fma_f32 v[236:237], v[108:109], v[238:239], v[116:117] op_sel:[1,1,0] op_sel_hi:[1,0,1] neg_lo:[1,0,0]\n"
    "v_pk_fma_f32 v[220:221], v[108:109], v[222:223], v[18:19] op_sel:[1,1,0] op_sel_hi:[1,0,1] neg_lo:[1,0,0]\n"
    "v_pk_fma_f32 v[116:117], v[108:109], v[236:237], v[234:235] op_sel_hi:[0,1,1]\n"
    "v_pk_fma_f32 v[18:19], v[108:109], v[220:221], v[218:219] op_sel_hi:[0,1,1]\n"
    "v_pk_fma_f32 v[234:235], v[108:109], v[236:237], v[116:117] op_sel:[1,1,0] op_sel_hi:[1,0,1] neg_lo:[1,0,0]\n"
    "v_pk_fma_f32 v[218:219], v[108:109], v[220:221], v[18:19] op_sel:[1,1,0] op_sel_hi:[1,0,1] neg_lo:[1,0,0]\n"
    "v_pk_fma_f32 v[116:117], v[108:109], v[234:235], v[232:233] op_sel_hi:[0,1,1]\n"
    "v_pk_fma_f32 v[18:19], v[108:109], v[218:219], v[216:217] op_sel_hi:[0,1,1]\n"
    "v_pk_fma_f32 v[232:233], v[108:109], v[234:235], v[116:117] op_sel:[1,1,0] op_sel_hi:[1,0,1] neg_lo:[1,0,0]\n"
    "v_pk_fma_f32 v[216:217], v[108:109], v[218:219], v[18:19] op_sel:[1,1,0] op_sel_hi:[1,0,1] neg_lo:[1,0,0]\n"
    "v_pk_fma_f32 v[116:117], v[108:109], v[232:233], v[230:231] op_sel_hi:[0,1,1]\n"
    "v_pk_fma_f32 v[18:19], v[108:109], v[216:217], v[214:215] op_sel_hi:[0,1,1]\n"
    "v_pk_fma_f32 v[230:231], v[108:109], v[232:233], v[116:117] op_sel:[1,1,0] op_sel_hi:[1,0,1] neg_lo:[1,0,0]\n"
    "v_pk_fma_f32 v[214:215], v[108:109], v[216:217], v[18:19] op_sel:[1,1,0] op_sel_hi:[1,0,1] neg_lo:[1,0,0]\n"
    "v_pk_fma_f32 v[116:117], v[108:109], v[230:231], v[228:229] op_sel_hi:[0,1,1]\n"
    "v_pk_fma_f32 v[18:19], v[108:109], v[214:215], v[212:213] op_sel_hi:[0,1,1]\n"
    "v_pk_fma_f32 v[228:229], v[108:109], v[230:231], v[116:117] op_sel:[1,1,0] op_sel_hi:[1,0,1] neg_lo:[1,0,0]\n"
    "v_pk_fma_f32 v[212:213], v[108:109], v[214:215], v[18:19] op_sel:[1,1,0] op_sel_hi:[1,0,1] neg_lo:[1,0,0]\n"
    "v_pk_fma_f32 v[116:117], v[108:109], v[228:229], v[226:227] op_sel_hi:[0,1,1]\n"
    "v_pk_fma_f32 v[18:19], v[108:109], v[212:213], v[210:211] op_sel_hi:[0,1,1]\n"
    "v_pk_fma_f32 v[226:227], v[108:109], v[228:229], v[116:117] op_sel:[1,1,0] op_sel_hi:[1,0,1] neg_lo:[1,0,0]\n"
    "v_pk_fma_f32 v[210:211], v[108:109], v[212:213], v[18:19] op_sel:[1,1,0] op_sel_hi:[1,0,1] neg_lo:[1,0,0]\n"
    "v_pk_fma_f32 v[116:117], v[108:109], v[226:227], v[224:225] op_sel_hi:[0,1,1]\n"
    "v_pk_fma_f32 v[18:19], v[108:109], v[210:211], v[208:209] op_sel_hi:[0,1,1]\n"
    "v_pk_fma_f32 v[224:225], v[108:109], v[226:227], v[116:117] op_sel:[1,1,0] op_sel_hi:[1,0,1] neg_lo:[1,0,0]\n"
    "v_pk_fma_f32 v[208:209], v[108:109], v[210:211], v[18:19] op_sel:[1,1,0] op_sel_hi:[1,0,1] neg_lo:[1,0,0]\n"
    "v_pk_fma_f32 v[116:117], v[110:111], v[224:225], v[208:209] op_sel_hi:[0,1,1]\n"
    "v_pk_fma_f32 v[210:211], v[110:111], v[224:225], v[116:117] op_sel:[1,1,0] op_sel_hi:[1,0,1] neg_lo:[1,0,0]\n"
    "v_mov_b32 v212, v210\n"
    "v_mov_b32 v213, v211\n"
    "s_nop 0\n"
    "v_permlane32_swap_b32 v210, v212\n"
    "v_permlane32_swap_b32 v211, v213\n"
    "v_pk_fma_f32 v[116:117], v[112:113], v[212:213], v[210:211] op_sel_hi:[0,1,1]\n"
    "v_pk_fma_f32 v[214:215], v[112:113], v[212:213], v[116:117] op_sel:[1,1,0] op_sel_hi:[1,0,1] neg_lo:[1,0,0]\n"
    "v_pk_fma_f32 v[116:117], v[118:119], v[214:215], v[38:39] op_sel_hi:[0,1,1]\n"
    "v_pk_fma_f32 v[216:217], v[118:119], v[214:215], v[116:117] op_sel:[1,1,0] op_sel_hi:[1,0,1] neg_lo:[1,0,0]\n"
    "v_pk_fma_f32 v[116:117], v[104:105], v[216:217], v[48:49] op_sel_hi:[0,1,1]\n"
    "v_pk_fma_f32 v[218:219], v[104:105], v[216:217], v[116:117] op_sel:[1,1,0] op_sel_hi:[1,0,1] neg_lo:[1,0,0]\n"
    "v_pk_mov_b32 v[220:221], v[216:217], v[218:219] op_sel:[0,0]\n"
    "v_pk_mov_b32 v[222:223], v[216:217], v[218:219] op_sel:[1,1]\n"
    "v_pk_fma_f32 v[116:117], v[102:103], v[220:221], v[144:145] op_sel_hi:[0,1,1]\n"
    "v_pk_fma_f32 v[18:19], v[102:103], v[222:223], v[128:129] op_sel_hi:[0,1,1]\n"
    "v_pk_fma_f32 v[144:145], v[102:103], v[222:223], v[116:117] op_sel:[1,0,0] op_sel_hi:[1,1,1] neg_lo:[1,0,0] neg_hi:[1,0,0]\n"
    "v_pk_fma_f32 v[128:129], v[102:103], v[220:221], v[18:19] op_sel:[1,0,0] op_sel_hi:[1,1,1]\n"
    "v_pk_fma_f32 v[116:117], v[102:103], v[144:145], v[146:147] op_sel_hi:[0,1,1]\n"
    "v_pk_fma_f32 v[18:19], v[102:103], v[128:129], v[130:131] op_sel_hi:[0,1,1]\n"
    "v_pk_fma_f32 v[146:147], v[102:103], v[128:129], v[116:117] op_sel:[1,0,0] op_sel_hi:[1,1,1] neg_lo:[1,0,0] neg_hi:[1,0,0]\n"
    "v_pk_fma_f32 v[130:131], v[102:103], v[144:145], v[18:19] op_sel:[1,0,0] op_sel_hi:[1,1,1]\n"
    "v_cvt_pk_bf16_f32 v224, v144, v145\n"
    "v_cvt_pk_bf16_f32 v232, -v128, -v129\n"
    "v_pk_fma_f32 v[116:117], v[102:103], v[146:147], v[148:149] op_sel_hi:[0,1,1]\n"
    "v_pk_fma_f32 v[18:19], v[102:103], v[130:131], v[132:133] op_sel_hi:[0,1,1]\n"
    "v_pk_fma_f32 v[148:149], v[102:103], v[130:131], v[116:117] op_sel:[1,0,0] op_sel_hi:[1,1,1] neg_lo:[1,0,0] neg_hi:[1,0,0]\n"
    "v_pk_fma_f32 v[132:133], v[102:103], v[146:147], v[18:19] op_sel:[1,0,0] op_sel_hi:[1,1,1]\n"
    "v_cvt_pk_bf16_f32 v225, v146, v147\n"
    "v_cvt_pk_bf16_f32 v233, -v130, -v131\n"
    "v_pk_fma_f32 v[116:117], v[102:103], v[148:149], v[150:151] op_sel_hi:[0,1,1]\n"
    "v_pk_fma_f32 v[18:19], v[102:103], v[132:133], v[134:135] op_sel_hi:[0,1,1]\n"
    "v_pk_fma_f32 v[150:151], v[102:103], v[132:133], v[116:117] op_sel:[1,0,0] op_sel_hi:[1,1,1] neg_lo:[1,0,0] neg_hi:[1,0,0]\n"
    "v_pk_fma_f32 v[134:135], v[102:103], v[148:149], v[18:19] op_sel:[1,0,0] op_sel_hi:[1,1,1]\n"
    "v_cvt_pk_bf16_f32 v226, v148, v149\n"
    "v_cvt_pk_bf16_f32 v234, -v132, -v133\n"
    "v_pk_fma_f32 v[116:117], v[102:103], v[150:151], v[152:153] op_sel_hi:[0,1,1]\n"
    "v_pk_fma_f32 v[18:19], v[102:103], v[134:135], v[136:137] op_sel_hi:[0,1,1]\n"
    "v_pk_fma_f32 v[152:153], v[102:103], v[134:135], v[116:117] op_sel:[1,0,0] op_sel_hi:[1,1,1] neg_lo:[1,0,0] neg_hi:[1,0,0]\n"
    "v_pk_fma_f32 v[136:137], v[102:103], v[150:151], v[18:19] op_sel:[1,0,0] op_sel_hi:[1,1,1]\n"
    "v_cvt_pk_bf16_f32 v227, v150, v151\n"
    "v_cvt_pk_bf16_f32 v235, -v134, -v135\n"
    "ds_write_b128 v63, v[224:227]\n"
    "ds_write_b128 v63, v[232:235] offset:2048\n"
    "v_pk_fma_f32 v[116:117], v[102:103], v[152:153], v[154:155] op_sel_hi:[0,1,1]\n"
    "v_pk_fma_f32 v[18:19], v[102:103], v[136:137], v[138:139] op_sel_hi:[0,1,1]\n"
    "v_pk_fma_f32 v[154:155], v[102:103], v[136:137], v[116:117] op_sel:[1,0,0] op_sel_hi:[1,1,1] neg_lo:[1,0,0] neg_hi:[1,0,0]\n"
    "v_pk_fma_f32 v[138:139], v[102:103], v[152:153], v[18:19] op_sel:[1,0,0] op_sel_hi:[1,1,1]\n"
    "v_cvt_pk_bf16_f32 v228, v152, v153\n"
    "v_cvt_pk_bf16_f32 v236, -v136, -v137\n"
    "v_pk_fma_f32 v[116:117], v[102:103], v[154:155], v[156:157] op_sel_hi:[0,1,1]\n"
    "v_pk_fma_f32 v[18:19], v[102:103], v[138:139], v[140:141] op_sel_hi:[0,1,1]\n"
    "v_pk_fma_f32 v[156:157], v[102:103], v[138:139], v[116:117] op_sel:[1,0,0] op_sel_hi:[1,1,1] neg_lo:[1,0,0] neg_hi:[1,0,0]\n"
    "v_pk_fma_f32 v[140:141], v[102:103], v[154:155], v[18:19] op_sel:[1,0,0] op_sel_hi:[1,1,1]\n"
    "v_cvt_pk_bf16_f32 v229, v154, v155\n"
    "v_cvt_pk_bf16_f32 v237, -v138, -v139\n"
    "v_pk_fma_f32 v[116:117], v[102:103], v[156:157], v[158:159] op_sel_hi:[0,1,1]\n"
    "v_pk_fma_f32 v[18:19], v[102:103], v[140:141], v[142:143] op_sel_hi:[0,1,1]\n"
    "v_pk_fma_f32 v[158:159], v[102:103], v[140:141], v[116:117] op_sel:[1,0,0] op_sel_hi:[1,1,1] neg_lo:[1,0,0] neg_hi:[1,0,0]\n"
    "v_pk_fma_f32 v[142:143], v[102:103], v[156:157], v[18:19] op_sel:[1,0,0] op_sel_hi:[1,1,1]\n"
    "v_cvt_pk_bf16_f32 v230, v156, v157\n"
    "v_cvt_pk_bf16_f32 v238, -v140, -v141\n"
    "v_cvt_pk_bf16_f32 v231, v158, v159\n"
    "v_cvt_pk_bf16_f32 v239, -v142, -v143\n"
    "ds_write_b128 v99, v[228:231]\n"
    "ds_write_b128 v99, v[236:239] offset:2048\n"
    "v_mfma_f32_32x32x16_bf16 v[192:207], a[176:179], a[160:163], 0\n"
    "s_add_u32 s46, s58, 8388608\n"
    "v_exp_f32 v176, v176\n"
    "v_exp_f32 v177, v177\n"
    "v_exp_f32 v178, v178\n"
    "v_exp_f32 v179, v179\n"
    "v_cvt_pk_bf16_f32 v120, v176, v177\n"
    "v_mfma_f32_32x32x16_bf16 v[160:175], a[140:143], v[124:127], v[160:175]\n"
    "global_load_lds_dwordx4 v81, s[32:33] offset:2048\n"
    "v_exp_f32 v180, v180\n"
    "v_exp_f32 v181, v181\n"
    "v_cvt_pk_bf16_f32 v121, v178, v179\n"
    "v_exp_f32 v182, v182\n"
    "v_exp_f32 v183, v183\n"
    "v_cvt_pk_bf16_f32 v122, v180, v181\n"
    "v_mfma_f32_32x32x16_bf16 v[192:207], a[180:183], a[164:167], v[192:207]\n"
    "global_load_dwordx4 a[128:131], v4, s[36:37] offset:0 sc1\n"
    "v_exp_f32 v184, v184\n"
    "v_exp_f32 v185, v185\n"
    "v_cvt_pk_bf16_f32 v123, v182, v183\n"
    "v_exp_f32 v186, v186\n"
    "v_exp_f32 v187, v187\n"
    "v_cvt_pk_bf16_f32 v124, v184, v185\n"
    "v_mfma_f32_32x32x16_bf16 a[208:223], a[184:187], v[120:123], a[208:223]\n"
    "s_waitcnt lgkmcnt(0)\n"
    "v_exp_f32 v188, v188\n"
    "v_exp_f32 v189, v189\n"
    "v_cvt_pk_bf16_f32 v125, v186, v187\n"
    "v_exp_f32 v190, v190\n"
    "v_exp_f32 v191, v191\n"
    "v_cvt_pk_bf16_f32 v126, v188, v189\n"
    "v_cvt_pk_bf16_f32 v127, v190, v191\n"
    "v_mfma_f32_32x32x16_bf16 v[176:191], a[176:179], a[168:171], 0\n"
    "ds_read_b64_tr_b16 v[208:209], v64 offset:0\n"
    "v_exp_f32 v192, v192\n"
    "v_exp_f32 v193, v193\n"
    "v_exp_f32 v194, v194\n"
    "v_exp_f32 v195, v195\n"
    "v_cvt_pk_bf16_f32 v120, v192, v193\n"
    "v_mfma_f32_32x32x16_bf16 a[208:223], a[188:191], v[124:127], a[208:223]\n"
    "ds_read_b64_tr_b16 v[210:211], v100 offset:256\n"
    "v_exp_f32 v196, v196\n"
    "v_exp_f32 v197, v197\n"
    "v_cvt_pk_bf16_f32 v121, v194, v195\n"
    "v_exp_f32 v198, v198\n"
    "v_exp_f32 v199, v199\n"
    "v_cvt_pk_bf16_f32 v122, v196, v197\n"
    "v_mfma_f32_32x32x16_bf16 v[176:191], a[180:183], a[172:175], v[176:191]\n"
    "global_load_dwordx4 a[132:135], v4, s[36:37] offset:1024 sc1\n"
    "v_exp_f32 v200, v200\n"
    "v_exp_f32 v201, v201\n"
    "v_cvt_pk_bf16_f32 v123, v198, v199\n"
    "v_exp_f32 v202, v202\n"
    "v_exp_f32 v203, v203\n"
    "v_cvt_pk_bf16_f32 v124, v200, v201\n"
    "v_mfma_f32_32x32x16_bf16 a[224:239], a[184:187], v[120:123], a[224:239]\n"
    "ds_read_b64_tr_b16 v[212:213], v64 offset:512\n"
    "v_exp_f32 v204, v204\n"
    "v_exp_f32 v205, v205\n"
    "v_cvt_pk_bf16_f32 v125, v202, v203\n"
    "v_exp_f32 v206, v206\n"
    "v_exp_f32 v207, v207\n"
    "v_cvt_pk_bf16_f32 v126, v204, v205\n"
    "v_cvt_pk_bf16_f32 v127, v206, v207\n"
    "v_mfma_f32_32x32x16_bf16 v[192:207], a[176:179], a[144:147], 0\n"
    "ds_read_b64_tr_b16 v[214:215], v100 offset:768\n"
    "v_exp_f32 v176, v176\n"
    "v_exp_f32 v177, v177\n"
    "v_exp_f32 v178, v178\n"
    "v_exp_f32 v179, v179\n"
    "v_cvt_pk_bf16_f32 v120, v176, v177\n"
    "v_mfma_f32_32x32x16_bf16 a[224:239], a[188:191], v[124:127], a[224:239]\n"
    "ds_read_b64_tr_b16 v[216:217], v64 offset:1024\n"
    "v_exp_f32 v180, v180\n"
    "v_exp_f32 v181, v181\n"
    "v_cvt_pk_bf16_f32 v121, v178, v179\n"
    "v_exp_f32 v182, v182\n"
    "v_exp_f32 v183, v183\n"
    "v_cvt_pk_bf16_f32 v122, v180, v181\n"
    "v_mfma_f32_32x32x16_bf16 v[192:207], a[180:183], a[148:151], v[192:207]\n"
    "global_load_dwordx4 a[136:139], v4, s[36:37] offset:2048 sc1\n"
    "v_exp_f32 v184, v184\n"
    "v_exp_f32 v185, v185\n"
    "v_cvt_pk_bf16_f32 v123, v182, v183\n"
    "v_exp_f32 v186, v186\n"
    "v_exp_f32 v187, v187\n"
    "v_cvt_pk_bf16_f32 v124, v184, v185\n"
    "v_mfma_f32_32x32x16_bf16 a[240:255], a[184:187], v[120:123], a[240:255]\n"
    "s_waitcnt vmcnt(16)\n"
    "v_exp_f32 v188, v188\n"
    "v_exp_f32 v189, v189\n"
    "v_cvt_pk_bf16_f32 v125, v186, v187\n"
    "v_exp_f32 v190, v190\n"
    "v_exp_f32 v191, v191\n"
    "v_cvt_pk_bf16_f32 v126, v188, v189\n"
    "v_cvt_pk_bf16_f32 v127, v190, v191\n"
    "v_mfma_f32_32x32x16_bf16 v[176:191], a[192:195], a[152:155], 0\n"
    "ds_read_b64_tr_b16 v[218:219], v100 offset:1280\n"
    "v_exp_f32 v192, v192\n"
    "v_exp_f32 v193, v193\n"
    "v_exp_f32 v194, v194\n"
    "v_exp_f32 v195, v195\n"
    "v_cvt_pk_bf16_f32 v120, v192, v193\n"
    "v_mfma_f32_32x32x16_bf16 a[240:255], a[188:191], v[124:127], a[240:255]\n"
    "ds_read_b64_tr_b16 v[220:221], v64 offset:1536\n"
    "v_exp_f32 v196, v196\n"
    "v_exp_f32 v197, v197\n"
    "v_cvt_pk_bf16_f32 v121, v194, v195\n"
    "v_exp_f32 v198, v198\n"
    "v_exp_f32 v199, v199\n"
    "v_cvt_pk_bf16_f32 v122, v196, v197\n"
    "v_mfma_f32_32x32x16_bf16 v[176:191], a[196:199], a[156:159], v[176:191]\n"
    "global_load_dwordx4 a[140:143], v4, s[36:37] offset:3072 sc1\n"
    "v_exp_f32 v200, v200\n"
    "v_exp_f32 v201, v201\n"
    "v_cvt_pk_bf16_f32 v123, v198, v199\n"
    "v_exp_f32 v202, v202\n"
    "v_exp_f32 v203, v203\n"
    "v_cvt_pk_bf16_f32 v124, v200, v201\n"
    "v_mfma_f32_32x32x16_bf16 v[160:175], a[184:187], v[120:123], v[160:175]\n"
    "ds_read_b64_tr_b16 v[222:223], v100 offset:1792\n"
    "v_exp_f32 v204, v204\n"
    "v_exp_f32 v205, v205\n"
    "v_cvt_pk_bf16_f32 v125, v202, v203\n"
    "v_exp_f32 v206, v206\n"
    "v_exp_f32 v207, v207\n"
    "v_cvt_pk_bf16_f32 v126, v204, v205\n"
    "v_cvt_pk_bf16_f32 v127, v206, v207\n"
    "v_mfma_f32_32x32x16_bf16 v[192:207], a[192:195], a[160:163], 0\n"
    "s_mov_b32 m0, s67\n"
    "v_exp_f32 v176, v176\n"
    "v_exp_f32 v177, v177\n"
    "v_exp_f32 v178, v178\n"
    "v_exp_f32 v179, v179\n"
    "v_cvt_pk_bf16_f32 v120, v176, v177\n"
    "v_mfma_f32_32x32x16_bf16 v[160:175], a[188:191], v[124:127], v[160:175]\n"
    "s_add_u32 s36, s36, 8192\n"
    "s_addc_u32 s37, s37, 0\n"
    "v_exp_f32 v180, v180\n"
    "v_exp_f32 v181, v181\n"
    "v_cvt_pk_bf16_f32 v121, v178, v179\n"
    "v_exp_f32 v182, v182\n"
    "v_exp_f32 v183, v183\n"
    "v_cvt_pk_bf16_f32 v122, v180, v181\n"
    "v_mfma_f32_32x32x16_bf16 v[192:207], a[196:199], a[164:167], v[192:207]\n"
    "global_load_dwordx4 a[176:179], v4, s[36:37] offset:-4096 sc1\n"
    "v_exp_f32 v184, v184\n"
    "v_exp_f32 v185, v185\n"
    "v_cvt_pk_bf16_f32 v123, v182, v183\n"
    "v_exp_f32 v186, v186\n"
    "v_exp_f32 v187, v187\n"
    "v_cvt_pk_bf16_f32 v124, v184, v185\n"
    "v_mfma_f32_32x32x16_bf16 a[208:223], a[200:203], v[120:123], a[208:223]\n"
    "s_waitcnt lgkmcnt(0)\n"
    "v_exp_f32 v188, v188\n"
    "v_exp_f32 v189, v189\n"
    "v_cvt_pk_bf16_f32 v125, v186, v187\n"
    "v_exp_f32 v190, v190\n"
    "v_exp_f32 v191, v191\n"
    "v_cvt_pk_bf16_f32 v126, v188, v189\n"
    "v_cvt_pk_bf16_f32 v127, v190, v191\n"
    "v_mfma_f32_32x32x16_bf16 v[176:191], a[192:195], a[168:171], 0\n"
    "buffer_store_dwordx4 v[208:211], v4, s[48:51], s45 offen offset:0 sc1\n"
    "v_exp_f32 v192, v192\n"
    "v_exp_f32 v193, v193\n"
    "v_exp_f32 v194, v194\n"
    "v_exp_f32 v195, v195\n"
    "v_cvt_pk_bf16_f32 v120, v192, v193\n"
    "v_mfma_f32_32x32x16_bf16 a[208:223], a[204:207], v[124:127], a[208:223]\n"
    "buffer_store_dwordx4 v[212:215], v4, s[48:51], s45 offen offset:1024 sc1\n"
    "v_exp_f32 v196, v196\n"
    "v_exp_f32 v197, v197\n"
    "v_cvt_pk_bf16_f32 v121, v194, v195\n"
    "v_exp_f32 v198, v198\n"
    "v_exp_f32 v199, v199\n"
    "v_cvt_pk_bf16_f32 v122, v196, v197\n"
    "v_mfma_f32_32x32x16_bf16 v[176:191], a[196:199], a[172:175], v[176:191]\n"
    "global_load_dwordx4 a[180:183], v4, s[36:37] offset:-3072 sc1\n"
    "v_exp_f32 v200, v200\n"
    "v_exp_f32 v201, v201\n"
    "v_cvt_pk_bf16_f32 v123, v198, v199\n"
    "v_exp_f32 v202, v202\n"
    "v_exp_f32 v203, v203\n"
    "v_cvt_pk_bf16_f32 v124, v200, v201\n"
    "v_mfma_f32_32x32x16_bf16 a[224:239], a[200:203], v[120:123], a[224:239]\n"
    "buffer_store_dwordx4 v[216:219], v4, s[48:51], s45 offen offset:2048 sc1\n"
    "v_exp_f32 v204, v204\n"
    "v_exp_f32 v205, v205\n"
    "v_cvt_pk_bf16_f32 v125, v202, v203\n"
    "v_exp_f32 v206, v206\n"
    "v_exp_f32 v207, v207\n"
    "v_cvt_pk_bf16_f32 v126, v204, v205\n"
    "v_cvt_pk_bf16_f32 v127, v206, v207\n"
    "v_mfma_f32_32x32x16_bf16 v[192:207], a[192:195], a[144:147], 0\n"
    "buffer_store_dwordx4 v[220:223], v65, s[48:51], s45 offen offset:3072 sc1\n"
    "v_exp_f32 v176, v176\n"
    "v_exp_f32 v177, v177\n"
    "v_exp_f32 v178, v178\n"
    "v_exp_f32 v179, v179\n"
    "v_cvt_pk_bf16_f32 v120, v176, v177\n"
    "v_mfma_f32_32x32x16_bf16 a[224:239], a[204:207], v[124:127], a[224:239]\n"
    "s_add_u32 s40, s40, s66\n"
    "s_addc_u32 s41, s41, 0\n"
    "v_exp_f32 v180, v180\n"
    "v_exp_f32 v181, v181\n"
    "v_cvt_pk_bf16_f32 v121, v178, v179\n"
    "v_exp_f32 v182, v182\n"
    "v_exp_f32 v183, v183\n"
    "v_cvt_pk_bf16_f32 v122, v180, v181\n"
    "v_mfma_f32_32x32x16_bf16 v[192:207], a[196:199], a[148:151], v[192:207]\n"
    "global_load_dwordx4 a[184:187], v4, s[36:37] offset:-2048 sc1\n"
    "v_exp_f32 v184, v184\n"
    "v_exp_f32 v185, v185\n"
    "v_cvt_pk_bf16_f32 v123, v182, v183\n"
    "v_exp_f32 v186, v186\n"
    "v_exp_f32 v187, v187\n"
    "v_cvt_pk_bf16_f32 v124, v184, v185\n"
    "v_mfma_f32_32x32x16_bf16 a[240:255], a[200:203], v[120:123], a[240:255]\n"
    "s_waitcnt vmcnt(13)\n"
    "v_exp_f32 v188, v188\n"
    "v_exp_f32 v189, v189\n"
    "v_cvt_pk_bf16_f32 v125, v186, v187\n"
    "v_exp_f32 v190, v190\n"
    "v_exp_f32 v191, v191\n"
    "v_cvt_pk_bf16_f32 v126, v188, v189\n"
    "v_cvt_pk_bf16_f32 v127, v190, v191\n"
    "v_mfma_f32_32x32x16_bf16 v[176:191], a[112:115], a[152:155], 0\n"
    "global_load_lds_dwordx4 v89, s[40:41] offset:0\n"
    "v_exp_f32 v192, v192\n"
    "v_exp_f32 v193, v193\n"
    "v_exp_f32 v194, v194\n"
    "v_exp_f32 v195, v195\n"
    "v_cvt_pk_bf16_f32 v120, v192, v193\n"
    "v_mfma_f32_32x32x16_bf16 a[240:255], a[204:207], v[124:127], a[240:255]\n"
    "s_add_u32 s58, s58, 8519680\n"
    "v_exp_f32 v196, v196\n"
    "v_exp_f32 v197, v197\n"
    "v_cvt_pk_bf16_f32 v121, v194, v195\n"
    "v_exp_f32 v198, v198\n"
    "v_exp_f32 v199, v199\n"
    "v_cvt_pk_bf16_f32 v122, v196, v197\n"
    "v_mfma_f32_32x32x16_bf16 v[176:191], a[116:119], a[156:159], v[176:191]\n"
    "global_load_dwordx4 a[188:191], v4, s[36:37] offset:-1024 sc1\n"
    "v_exp_f32 v200, v200\n"
    "v_exp_f32 v201, v201\n"
    "v_cvt_pk_bf16_f32 v123, v198, v199\n"
    "v_exp_f32 v202, v202\n"
    "v_exp_f32 v203, v203\n"
    "v_cvt_pk_bf16_f32 v124, v200, v201\n"
    "v_mfma_f32_32x32x16_bf16 v[160:175], a[200:203], v[120:123], v[160:175]\n"
    "s_lshl1_add_u32 s56, s64, s74\n"
    "v_exp_f32 v204, v204\n"
    "v_exp_f32 v205, v205\n"
    "v_cvt_pk_bf16_f32 v125, v202, v203\n"
    "v_exp_f32 v206, v206\n"
    "v_exp_f32 v207, v207\n"
    "v_cvt_pk_bf16_f32 v126, v204, v205\n"
    "v_cvt_pk_bf16_f32 v127, v206, v207\n"
    "v_mfma_f32_32x32x16_bf16 v[192:207], a[112:115], a[160:163], 0\n"
    "s_lshl_b32 s59, s56, 14\n"
    "v_exp_f32 v176, v176\n"
    "v_exp_f32 v177, v177\n"
    "v_exp_f32 v178, v178\n"
    "v_exp_f32 v179, v179\n"
    "v_cvt_pk_bf16_f32 v120, v176, v177\n"
    "v_mfma_f32_32x32x16_bf16 v[160:175], a[204:207], v[124:127], v[160:175]\n"
    "global_load_lds_dwordx4 v92, s[40:41] offset:1024\n"
    "v_exp_f32 v180, v180\n"
    "v_exp_f32 v181, v181\n"
    "v_cvt_pk_bf16_f32 v121, v178, v179\n"
    "v_exp_f32 v182, v182\n"
    "v_exp_f32 v183, v183\n"
    "v_cvt_pk_bf16_f32 v122, v180, v181\n"
    "v_mfma_f32_32x32x16_bf16 v[192:207], a[116:119], a[164:167], v[192:207]\n"
    "global_load_dwordx4 a[192:195], v4, s[36:37] offset:0 sc1\n"
    "v_exp_f32 v184, v184\n"
    "v_exp_f32 v185, v185\n"
    "v_cvt_pk_bf16_f32 v123, v182, v183\n"
    "v_exp_f32 v186, v186\n"
    "v_exp_f32 v187, v187\n"
    "v_cvt_pk_bf16_f32 v124, v184, v185\n"
    "v_mfma_f32_32x32x16_bf16 a[208:223], a[120:123], v[120:123], a[208:223]\n"
    "s_add_u32 s59, s59, 4194304\n"
    "v_exp_f32 v188, v188\n"
    "v_exp_f32 v189, v189\n"
    "v_cvt_pk_bf16_f32 v125, v186, v187\n"
    "v_exp_f32 v190, v190\n"
    "v_exp_f32 v191, v191\n"
    "v_cvt_pk_bf16_f32 v126, v188, v189\n"
    "v_cvt_pk_bf16_f32 v127, v190, v191\n"
    "v_mfma_f32_32x32x16_bf16 v[176:191], a[112:115], a[168:171], 0\n"
    "s_lshl_b32 s56, s56, 10\n"
    "v_exp_f32 v192, v192\n"
    "v_exp_f32 v193, v193\n"
    "v_exp_f32 v194, v194\n"
    "v_exp_f32 v195, v195\n"
    "v_cvt_pk_bf16_f32 v120, v192, v193\n"
    "v_mfma_f32_32x32x16_bf16 a[208:223], a[124:127], v[124:127], a[208:223]\n"
    "s_add_u32 s56, s56, 8519680\n"
    "v_exp_f32 v196, v196\n"
    "v_exp_f32 v197, v197\n"
    "v_cvt_pk_bf16_f32 v121, v194, v195\n"
    "v_exp_f32 v198, v198\n"
    "v_exp_f32 v199, v199\n"
    "v_cvt_pk_bf16_f32 v122, v196, v197\n"
    "v_mfma_f32_32x32x16_bf16 v[176:191], a[116:119], a[172:175], v[176:191]\n"
    "global_load_dwordx4 a[196:199], v4, s[36:37] offset:1024 sc1\n"
    "v_exp_f32 v200, v200\n"
    "v_exp_f32 v201, v201\n"
    "v_cvt_pk_bf16_f32 v123, v198, v199\n"
    "v_exp_f32 v202, v202\n"
    "v_exp_f32 v203, v203\n"
    "v_cvt_pk_bf16_f32 v124, v200, v201\n"
    "v_mfma_f32_32x32x16_bf16 a[224:239], a[120:123], v[120:123], a[224:239]\n"
    "global_load_lds_dwordx4 v93, s[40:41] offset:2048\n"
    "v_exp_f32 v204, v204\n"
    "v_exp_f32 v205, v205\n"
    "v_cvt_pk_bf16_f32 v125, v202, v203\n"
    "v_exp_f32 v206, v206\n"
    "v_exp_f32 v207, v207\n"
    "v_cvt_pk_bf16_f32 v126, v204, v205\n"
    "v_cvt_pk_bf16_f32 v127, v206, v207\n"
    "v_mfma_f32_32x32x16_bf16 v[192:207], a[112:115], a[144:147], 0\n"
    "s_and_b32 s64, s46, 4294963200\n"
    "v_exp_f32 v176, v176\n"
    "v_exp_f32 v177, v177\n"
    "v_exp_f32 v178, v178\n"
    "v_exp_f32 v179, v179\n"
    "v_cvt_pk_bf16_f32 v120, v176, v177\n"
    "v_mfma_f32_32x32x16_bf16 a[224:239], a[124:127], v[124:127], a[224:239]\n"
    "s_add_u32 s65, s28, s92\n"
    "v_exp_f32 v180, v180\n"
    "v_exp_f32 v181, v181\n"
    "v_cvt_pk_bf16_f32 v121, v178, v179\n"
    "v_exp_f32 v182, v182\n"
    "v_exp_f32 v183, v183\n"
    "v_cvt_pk_bf16_f32 v122, v180, v181\n"
    "v_mfma_f32_32x32x16_bf16 v[192:207], a[116:119], a[148:151], v[192:207]\n"
    "global_load_dwordx4 a[200:203], v4, s[36:37] offset:2048 sc1\n"
    "v_exp_f32 v184, v184\n"
    "v_exp_f32 v185, v185\n"
    "v_cvt_pk_bf16_f32 v123, v182, v183\n"
    "v_exp_f32 v186, v186\n"
    "v_exp_f32 v187, v187\n"
    "v_cvt_pk_bf16_f32 v124, v184, v185\n"
    "v_mfma_f32_32x32x16_bf16 a[240:255], a[120:123], v[120:123], a[240:255]\n"
    "s_waitcnt vmcnt(14)\n"
    "v_exp_f32 v188, v188\n"
    "v_exp_f32 v189, v189\n"
    "v_cvt_pk_bf16_f32 v125, v186, v187\n"
    "v_exp_f32 v190, v190\n"
    "v_exp_f32 v191, v191\n"
    "v_cvt_pk_bf16_f32 v126, v188, v189\n"
    "v_cvt_pk_bf16_f32 v127, v190, v191\n"
    "v_mfma_f32_32x32x16_bf16 v[176:191], a[128:131], a[152:155], 0\n"
    "global_load_lds_dwordx4 v94, s[40:41] offset:3072\n"
    "v_exp_f32 v192, v192\n"
    "v_exp_f32 v193, v193\n"
    "v_exp_f32 v194, v194\n"
    "v_exp_f32 v195, v195\n"
    "v_cvt_pk_bf16_f32 v120, v192, v193\n"
    "v_mfma_f32_32x32x16_bf16 a[240:255], a[124:127], v[124:127], a[240:255]\n"
    "s_add_u32 s84, s72, 0\n"
    "v_exp_f32 v196, v196\n"
    "v_exp_f32 v197, v197\n"
    "v_cvt_pk_bf16_f32 v121, v194, v195\n"
    "v_exp_f32 v198, v198\n"
    "v_exp_f32 v199, v199\n"
    "v_cvt_pk_bf16_f32 v122, v196, v197\n"
    "v_mfma_f32_32x32x16_bf16 v[176:191], a[132:135], a[156:159], v[176:191]\n"
    "global_load_dwordx4 a[204:207], v4, s[36:37] offset:3072 sc1\n"
    "v_exp_f32 v200, v200\n"
    "v_exp_f32 v201, v201\n"
    "v_cvt_pk_bf16_f32 v123, v198, v199\n"
    "v_exp_f32 v202, v202\n"
    "v_exp_f32 v203, v203\n"
    "v_cvt_pk_bf16_f32 v124, v200, v201\n"
    "v_mfma_f32_32x32x16_bf16 v[160:175], a[120:123], v[120:123], v[160:175]\n"
    "s_and_b32 s84, s84, 3\n"
    "v_exp_f32 v204, v204\n"
    "v_exp_f32 v205, v205\n"
    "v_cvt_pk_bf16_f32 v125, v202, v203\n"
    "v_exp_f32 v206, v206\n"
    "v_exp_f32 v207, v207\n"
    "v_cvt_pk_bf16_f32 v126, v204, v205\n"
    "v_cvt_pk_bf16_f32 v127, v206, v207\n"
    "v_mfma_f32_32x32x16_bf16 v[192:207], a[128:131], a[160:163], 0\n"
    "s_mul_i32 s84, s84, 12288\n"
    "v_exp_f32 v176, v176\n"
    "v_exp_f32 v177, v177\n"
    "v_exp_f32 v178, v178\n"
    "v_exp_f32 v179, v179\n"
    "v_cvt_pk_bf16_f32 v120, v176, v177\n"
    "v_mfma_f32_32x32x16_bf16 v[160:175], a[124:127], v[124:127], v[160:175]\n"
    "s_add_u32 s36, s36, 8192\n"
    "s_addc_u32 s37, s37, 0\n"
    "v_exp_f32 v180, v180\n"
    "v_exp_f32 v181, v181\n"
    "v_cvt_pk_bf16_f32 v121, v178, v179\n"
    "v_exp_f32 v182, v182\n"
    "v_exp_f32 v183, v183\n"
    "v_cvt_pk_bf16_f32 v122, v180, v181\n"
    "v_mfma_f32_32x32x16_bf16 v[192:207], a[132:135], a[164:167], v[192:207]\n"
    "global_load_dwordx4 a[112:115], v4, s[36:37] offset:-4096 sc1\n"
    "v_exp_f32 v184, v184\n"
    "v_exp_f32 v185, v185\n"
    "v_cvt_pk_bf16_f32 v123, v182, v183\n"
    "v_exp_f32 v186, v186\n"
    "v_exp_f32 v187, v187\n"
    "v_cvt_pk_bf16_f32 v124, v184, v185\n"
    "v_mfma_f32_32x32x16_bf16 a[208:223], a[136:139], v[120:123], a[208:223]\n"
    "s_add_u32 m0, m0, 4096\n"
    "v_exp_f32 v188, v188\n"
    "v_exp_f32 v189, v189\n"
    "v_cvt_pk_bf16_f32 v125, v186, v187\n"
    "v_exp_f32 v190, v190\n"
    "v_exp_f32 v191, v191\n"
    "v_cvt_pk_bf16_f32 v126, v188, v189\n"
    "v_cvt_pk_bf16_f32 v127, v190, v191\n"
    "v_mfma_f32_32x32x16_bf16 v[176:191], a[128:131], a[168:171], 0\n"
    "s_add_u32 s40, s40, 28672\n"
    "s_addc_u32 s41, s41, 0\n"
    "v_exp_f32 v192, v192\n"
    "v_exp_f32 v193, v193\n"
    "v_exp_f32 v194, v194\n"
    "v_exp_f32 v195, v195\n"
    "v_cvt_pk_bf16_f32 v120, v192, v193\n"
    "v_mfma_f32_32x32x16_bf16 a[208:223], a[140:143], v[124:127], a[208:223]\n"
    "global_load_lds_dwordx4 v89, s[40:41] offset:0\n"
    "v_exp_f32 v196, v196\n"
    "v_exp_f32 v197, v197\n"
    "v_cvt_pk_bf16_f32 v121, v194, v195\n"
    "v_exp_f32 v198, v198\n"
    "v_exp_f32 v199, v199\n"
    "v_cvt_pk_bf16_f32 v122, v196, v197\n"
    "v_mfma_f32_32x32x16_bf16 v[176:191], a[132:135], a[172:175], v[176:191]\n"
    "global_load_dwordx4 a[116:119], v4, s[36:37] offset:-3072 sc1\n"
    "v_exp_f32 v200, v200\n"
    "v_exp_f32 v201, v201\n"
    "v_cvt_pk_bf16_f32 v123, v198, v199\n"
    "v_exp_f32 v202, v202\n"
    "v_exp_f32 v203, v203\n"
    "v_cvt_pk_bf16_f32 v124, v200, v201\n"
    "v_mfma_f32_32x32x16_bf16 a[224:239], a[136:139], v[120:123], a[224:239]\n"
    "s_add_u32 s84, s84, 57344\n"
    "v_add_u32 v71, s84, v4\n"
    "v_exp_f32 v204, v204\n"
    "v_exp_f32 v205, v205\n"
    "v_cvt_pk_bf16_f32 v125, v202, v203\n"
    "v_exp_f32 v206, v206\n"
    "v_exp_f32 v207, v207\n"
    "v_cvt_pk_bf16_f32 v126, v204, v205\n"
    "v_cvt_pk_bf16_f32 v127, v206, v207\n"
    "v_mfma_f32_32x32x16_bf16 v[192:207], a[128:131], a[144:147], 0\n"
    "s_waitcnt vmcnt(13)\n"
    "v_exp_f32 v176, v176\n"
    "v_exp_f32 v177, v177\n"
    "v_exp_f32 v178, v178\n"
    "v_exp_f32 v179, v179\n"
    "v_cvt_pk_bf16_f32 v120, v176, v177\n"
    "v_mfma_f32_32x32x16_bf16 a[224:239], a[140:143], v[124:127], a[224:239]\n"
    "buffer_store_dwordx4 v[30:33], v52, s[48:51], s46 offen sc1\n"
    "v_exp_f32 v180, v180\n"
    "v_exp_f32 v181, v181\n"
    "v_cvt_pk_bf16_f32 v121, v178, v179\n"
    "v_exp_f32 v182, v182\n"
    "v_exp_f32 v183, v183\n"
    "v_cvt_pk_bf16_f32 v122, v180, v181\n"
    "v_mfma_f32_32x32x16_bf16 v[192:207], a[132:135], a[148:151], v[192:207]\n"
    "global_load_dwordx4 a[120:123], v4, s[36:37] offset:-2048 sc1\n"
    "v_exp_f32 v184, v184\n"
    "v_exp_f32 v185, v185\n"
    "v_cvt_pk_bf16_f32 v123, v182, v183\n"
    "v_exp_f32 v186, v186\n"
    "v_exp_f32 v187, v187\n"
    "v_cvt_pk_bf16_f32 v124, v184, v185\n"
    "v_mfma_f32_32x32x16_bf16 a[240:255], a[136:139], v[120:123], a[240:255]\n"
    "s_waitcnt vmcnt(12)\n"
    "v_exp_f32 v188, v188\n"
    "v_exp_f32 v189, v189\n"
    "v_cvt_pk_bf16_f32 v125, v186, v187\n"
    "v_exp_f32 v190, v190\n"
    "v_exp_f32 v191, v191\n"
    "v_cvt_pk_bf16_f32 v126, v188, v189\n"
    "v_cvt_pk_bf16_f32 v127, v190, v191\n"
    "v_mfma_f32_32x32x16_bf16 v[176:191], a[176:179], a[152:155], 0\n"
    "buffer_load_dwordx4 v[144:147], v67, s[8:11], s65 offen offset:0\n"
    "v_exp_f32 v192, v192\n"
    "v_exp_f32 v193, v193\n"
    "v_exp_f32 v194, v194\n"
    "v_exp_f32 v195, v195\n"
    "v_cvt_pk_bf16_f32 v120, v192, v193\n"
    "v_mfma_f32_32x32x16_bf16 a[240:255], a[140:143], v[124:127], a[240:255]\n"
    "global_load_lds_dwordx4 v92, s[40:41] offset:1024\n"
    "v_exp_f32 v196, v196\n"
    "v_exp_f32 v197, v197\n"
    "v_cvt_pk_bf16_f32 v121, v194, v195\n"
    "v_exp_f32 v198, v198\n"
    "v_exp_f32 v199, v199\n"
    "v_cvt_pk_bf16_f32 v122, v196, v197\n"
    "v_mfma_f32_32x32x16_bf16 v[176:191], a[180:183], a[156:159], v[176:191]\n"
    "global_load_dwordx4 a[124:127], v4, s[36:37] offset:-1024 sc1\n"
    "v_exp_f32 v200, v200\n"
    "v_exp_f32 v201, v201\n"
    "v_cvt_pk_bf16_f32 v123, v198, v199\n"
    "v_exp_f32 v202, v202\n"
    "v_exp_f32 v203, v203\n"
    "v_cvt_pk_bf16_f32 v124, v200, v201\n"
    "v_mfma_f32_32x32x16_bf16 v[160:175], a[136:139], v[120:123], v[160:175]\n"
    "s_add_u32 s84, s72, 1\n"
    "v_exp_f32 v204, v204\n"
    "v_exp_f32 v205, v205\n"
    "v_cvt_pk_bf16_f32 v125, v202, v203\n"
    "v_exp_f32 v206, v206\n"
    "v_exp_f32 v207, v207\n"
    "v_cvt_pk_bf16_f32 v126, v204, v205\n"
    "v_cvt_pk_bf16_f32 v127, v206, v207\n"
    "v_mfma_f32_32x32x16_bf16 v[192:207], a[176:179], a[160:163], 0\n"
    "s_and_b32 s84, s84, 3\n"
    "v_exp_f32 v176, v176\n"
    "v_exp_f32 v177, v177\n"
    "v_exp_f32 v178, v178\n"
    "v_exp_f32 v179, v179\n"
    "v_cvt_pk_bf16_f32 v120, v176, v177\n"
    "v_mfma_f32_32x32x16_bf16 v[160:175], a[140:143], v[124:127], v[160:175]\n"
    "s_mul_i32 s84, s84, 12288\n"
    "v_exp_f32 v180, v180\n"
    "v_exp_f32 v181, v181\n"
    "v_cvt_pk_bf16_f32 v121, v178, v179\n"
    "v_exp_f32 v182, v182\n"
    "v_exp_f32 v183, v183\n"
    "v_cvt_pk_bf16_f32 v122, v180, v181\n"
    "v_mfma_f32_32x32x16_bf16 v[192:207], a[180:183], a[164:167], v[192:207]\n"
    "global_load_dwordx4 a[128:131], v4, s[36:37] offset:0 sc1\n"
    "v_exp_f32 v184, v184\n"
    "v_exp_f32 v185, v185\n"
    "v_cvt_pk_bf16_f32 v123, v182, v183\n"
    "v_exp_f32 v186, v186\n"
    "v_exp_f32 v187, v187\n"
    "v_cvt_pk_bf16_f32 v124, v184, v185\n"
    "v_mfma_f32_32x32x16_bf16 a[208:223], a[184:187], v[120:123], a[208:223]\n"
    "buffer_load_dwordx4 v[148:151], v67, s[8:11], s65 offen offset:16\n"
    "v_exp_f32 v188, v188\n"
    "v_exp_f32 v189, v189\n"
    "v_cvt_pk_bf16_f32 v125, v186, v187\n"
    "v_exp_f32 v190, v190\n"
    "v_exp_f32 v191, v191\n"
    "v_cvt_pk_bf16_f32 v126, v188, v189\n"
    "v_cvt_pk_bf16_f32 v127, v190, v191\n"
    "v_mfma_f32_32x32x16_bf16 v[176:191], a[176:179], a[168:171], 0\n"
    "global_load_lds_dwordx4 v93, s[40:41] offset:2048\n"
    "v_exp_f32 v192, v192\n"
    "v_exp_f32 v193, v193\n"
    "v_exp_f32 v194, v194\n"
    "v_exp_f32 v195, v195\n"
    "v_cvt_pk_bf16_f32 v120, v192, v193\n"
    "v_mfma_f32_32x32x16_bf16 a[208:223], a[188:191], v[124:127], a[208:223]\n"
    "s_add_u32 s84, s84, 57344\n"
    "v_add_u32 v72, s84, v4\n"
    "v_exp_f32 v196, v196\n"
    "v_exp_f32 v197, v197\n"
    "v_cvt_pk_bf16_f32 v121, v194, v195\n"
    "v_exp_f32 v198, v198\n"
    "v_exp_f32 v199, v199\n"
    "v_cvt_pk_bf16_f32 v122, v196, v197\n"
    "v_mfma_f32_32x32x16_bf16 v[176:191], a[180:183], a[172:175], v[176:191]\n"
    "global_load_dwordx4 a[132:135], v4, s[36:37] offset:1024 sc1\n"
    "v_exp_f32 v200, v200\n"
    "v_exp_f32 v201, v201\n"
    "v_cvt_pk_bf16_f32 v123, v198, v199\n"
    "v_exp_f32 v202, v202\n"
    "v_exp_f32 v203, v203\n"
    "v_cvt_pk_bf16_f32 v124, v200, v201\n"
    "v_mfma_f32_32x32x16_bf16 a[224:239], a[184:187], v[120:123], a[224:239]\n"
    "s_add_u32 s84, s72, 2\n"
    "v_exp_f32 v204, v204\n"
    "v_exp_f32 v205, v205\n"
    "v_cvt_pk_bf16_f32 v125, v202, v203\n"
    "v_exp_f32 v206, v206\n"
    "v_exp_f32 v207, v207\n"
    "v_cvt_pk_bf16_f32 v126, v204, v205\n"
    "v_cvt_pk_bf16_f32 v127, v206, v207\n"
    "v_mfma_f32_32x32x16_bf16 v[192:207], a[176:179], a[144:147], 0\n"
    "s_and_b32 s84, s84, 3\n"
    "v_exp_f32 v176, v176\n"
    "v_exp_f32 v177, v177\n"
    "v_exp_f32 v178, v178\n"
    "v_exp_f32 v179, v179\n"
    "v_cvt_pk_bf16_f32 v120, v176, v177\n"
    "v_mfma_f32_32x32x16_bf16 a[224:239], a[188:191], v[124:127], a[224:239]\n"
    "buffer_load_dwordx2 v[152:153], v68, s[68:71], s28 offen offset:0\n"
    "v_exp_f32 v180, v180\n"
    "v_exp_f32 v181, v181\n"
    "v_cvt_pk_bf16_f32 v121, v178, v179\n"
    "v_exp_f32 v182, v182\n"
    "v_exp_f32 v183, v183\n"
    "v_cvt_pk_bf16_f32 v122, v180, v181\n"
    "v_mfma_f32_32x32x16_bf16 v[192:207], a[180:183], a[148:151], v[192:207]\n"
    "global_load_dwordx4 a[136:139], v4, s[36:37] offset:2048 sc1\n"
    "v_exp_f32 v184, v184\n"
    "v_exp_f32 v185, v185\n"
    "v_cvt_pk_bf16_f32 v123, v182, v183\n"
    "v_exp_f32 v186, v186\n"
    "v_exp_f32 v187, v187\n"
    "v_cvt_pk_bf16_f32 v124, v184, v185\n"
    "v_mfma_f32_32x32x16_bf16 a[240:255], a[184:187], v[120:123], a[240:255]\n"
    "s_waitcnt vmcnt(14)\n"
    "v_exp_f32 v188, v188\n"
    "v_exp_f32 v189, v189\n"
    "v_cvt_pk_bf16_f32 v125, v186, v187\n"
    "v_exp_f32 v190, v190\n"
    "v_exp_f32 v191, v191\n"
    "v_cvt_pk_bf16_f32 v126, v188, v189\n"
    "v_cvt_pk_bf16_f32 v127, v190, v191\n"
    "v_mfma_f32_32x32x16_bf16 v[176:191], a[192:195], a[152:155], 0\n"
    "buffer_load_dwordx2 v[154:155], v68, s[68:71], s28 offen offset:16\n"
    "v_exp_f32 v192, v192\n"
    "v_exp_f32 v193, v193\n"
    "v_exp_f32 v194, v194\n"
    "v_exp_f32 v195, v195\n"
    "v_cvt_pk_bf16_f32 v120, v192, v193\n"
    "v_mfma_f32_32x32x16_bf16 a[240:255], a[188:191], v[124:127], a[240:255]\n"
    "buffer_load_dwordx2 v[156:157], v68, s[68:71], s28 offen offset:32\n"
    "v_exp_f32 v196, v196\n"
    "v_exp_f32 v197, v197\n"
    "v_cvt_pk_bf16_f32 v121, v194, v195\n"
    "v_exp_f32 v198, v198\n"
    "v_exp_f32 v199, v199\n"
    "v_cvt_pk_bf16_f32 v122, v196, v197\n"
    "v_mfma_f32_32x32x16_bf16 v[176:191], a[196:199], a[156:159], v[176:191]\n"
    "global_load_dwordx4 a[140:143], v4, s[36:37] offset:3072 sc1\n"
    "v_exp_f32 v200, v200\n"
    "v_exp_f32 v201, v201\n"
    "v_cvt_pk_bf16_f32 v123, v198, v199\n"
    "v_exp_f32 v202, v202\n"
    "v_exp_f32 v203, v203\n"
    "v_cvt_pk_bf16_f32 v124, v200, v201\n"
    "v_mfma_f32_32x32x16_bf16 v[160:175], a[184:187], v[120:123], v[160:175]\n"
    "buffer_load_dwordx2 v[158:159], v68, s[68:71], s28 offen offset:48\n"
    "v_exp_f32 v204, v204\n"
    "v_exp_f32 v205, v205\n"
    "v_cvt_pk_bf16_f32 v125, v202, v203\n"
    "v_exp_f32 v206, v206\n"
    "v_exp_f32 v207, v207\n"
    "v_cvt_pk_bf16_f32 v126, v204, v205\n"
    "v_cvt_pk_bf16_f32 v127, v206, v207\n"
    "v_mfma_f32_32x32x16_bf16 v[192:207], a[192:195], a[160:163], 0\n"
    "s_add_u32 s20, s48, s47\n"
    "s_addc_u32 s21, s49, 0\n"
    "v_exp_f32 v176, v176\n"
    "v_exp_f32 v177, v177\n"
    "v_exp_f32 v178, v178\n"
    "v_exp_f32 v179, v179\n"
    "v_cvt_pk_bf16_f32 v120, v176, v177\n"
    "v_mfma_f32_32x32x16_bf16 v[160:175], a[188:191], v[124:127], v[160:175]\n"
    "s_add_u32 s36, s36, 8192\n"
    "s_addc_u32 s37, s37, 0\n"
    "v_exp_f32 v180, v180\n"
    "v_exp_f32 v181, v181\n"
    "v_cvt_pk_bf16_f32 v121, v178, v179\n"
    "v_exp_f32 v182, v182\n"
    "v_exp_f32 v183, v183\n"
    "v_cvt_pk_bf16_f32 v122, v180, v181\n"
    "v_mfma_f32_32x32x16_bf16 v[192:207], a[196:199], a[164:167], v[192:207]\n"
    "global_load_dwordx4 a[176:179], v4, s[36:37] offset:-4096 sc1\n"
    "v_exp_f32 v184, v184\n"
    "v_exp_f32 v185, v185\n"
    "v_cvt_pk_bf16_f32 v123, v182, v183\n"
    "v_exp_f32 v186, v186\n"
    "v_exp_f32 v187, v187\n"
    "v_cvt_pk_bf16_f32 v124, v184, v185\n"
    "v_mfma_f32_32x32x16_bf16 a[208:223], a[200:203], v[120:123], a[208:223]\n"
    "global_load_lds_dwordx4 v94, s[40:41] offset:3072\n"
    "v_exp_f32 v188, v188\n"
    "v_exp_f32 v189, v189\n"
    "v_cvt_pk_bf16_f32 v125, v186, v187\n"
    "v_exp_f32 v190, v190\n"
    "v_exp_f32 v191, v191\n"
    "v_cvt_pk_bf16_f32 v126, v188, v189\n"
    "v_cvt_pk_bf16_f32 v127, v190, v191\n"
    "v_mfma_f32_32x32x16_bf16 v[176:191], a[192:195], a[168:171], 0\n"
    "s_mul_i32 s84, s84, 12288\n"
    "v_exp_f32 v192, v192\n"
    "v_exp_f32 v193, v193\n"
    "v_exp_f32 v194, v194\n"
    "v_exp_f32 v195, v195\n"
    "v_cvt_pk_bf16_f32 v120, v192, v193\n"
    "v_mfma_f32_32x32x16_bf16 a[208:223], a[204:207], v[124:127], a[208:223]\n"
    "s_add_u32 s84, s84, 61440\n"
    "v_add_u32 v73, s84, v4\n"
    "v_exp_f32 v196, v196\n"
    "v_exp_f32 v197, v197\n"
    "v_cvt_pk_bf16_f32 v121, v194, v195\n"
    "v_exp_f32 v198, v198\n"
    "v_exp_f32 v199, v199\n"
    "v_cvt_pk_bf16_f32 v122, v196, v197\n"
    "v_mfma_f32_32x32x16_bf16 v[176:191], a[196:199], a[172:175], v[176:191]\n"
    "global_load_dwordx4 a[180:183], v4, s[36:37] offset:-3072 sc1\n"
    "v_exp_f32 v200, v200\n"
    "v_exp_f32 v201, v201\n"
    "v_cvt_pk_bf16_f32 v123, v198, v199\n"
    "v_exp_f32 v202, v202\n"
    "v_exp_f32 v203, v203\n"
    "v_cvt_pk_bf16_f32 v124, v200, v201\n"
    "v_mfma_f32_32x32x16_bf16 a[224:239], a[200:203], v[120:123], a[224:239]\n"
    "s_add_u32 s84, s72, 3\n"
    "v_exp_f32 v204, v204\n"
    "v_exp_f32 v205, v205\n"
    "v_cvt_pk_bf16_f32 v125, v202, v203\n"
    "v_exp_f32 v206, v206\n"
    "v_exp_f32 v207, v207\n"
    "v_cvt_pk_bf16_f32 v126, v204, v205\n"
    "v_cvt_pk_bf16_f32 v127, v206, v207\n"
    "v_mfma_f32_32x32x16_bf16 v[192:207], a[192:195], a[144:147], 0\n"
    "s_add_u32 m0, m0, 4096\n"
    "v_exp_f32 v176, v176\n"
    "v_exp_f32 v177, v177\n"
    "v_exp_f32 v178, v178\n"
    "v_exp_f32 v179, v179\n"
    "v_cvt_pk_bf16_f32 v120, v176, v177\n"
    "v_mfma_f32_32x32x16_bf16 a[224:239], a[204:207], v[124:127], a[224:239]\n"
    "s_add_u32 s40, s40, 28672\n"
    "s_addc_u32 s41, s41, 0\n"
    "v_exp_f32 v180, v180\n"
    "v_exp_f32 v181, v181\n"
    "v_cvt_pk_bf16_f32 v121, v178, v179\n"
    "v_exp_f32 v182, v182\n"
    "v_exp_f32 v183, v183\n"
    "v_cvt_pk_bf16_f32 v122, v180, v181\n"
    "v_mfma_f32_32x32x16_bf16 v[192:207], a[196:199], a[148:151], v[192:207]\n"
    "global_load_dwordx4 a[184:187], v4, s[36:37] offset:-2048 sc1\n"
    "v_exp_f32 v184, v184\n"
    "v_exp_f32 v185, v185\n"
    "v_cvt_pk_bf16_f32 v123, v182, v183\n"
    "v_exp_f32 v186, v186\n"
    "v_exp_f32 v187, v187\n"
    "v_cvt_pk_bf16_f32 v124, v184, v185\n"
    "v_mfma_f32_32x32x16_bf16 a[240:255], a[200:203], v[120:123], a[240:255]\n"
    "s_waitcnt vmcnt(14)\n"
    "v_exp_f32 v188, v188\n"
    "v_exp_f32 v189, v189\n"
    "v_cvt_pk_bf16_f32 v125, v186, v187\n"
    "v_exp_f32 v190, v190\n"
    "v_exp_f32 v191, v191\n"
    "v_cvt_pk_bf16_f32 v126, v188, v189\n"
    "v_cvt_pk_bf16_f32 v127, v190, v191\n"
    "v_mfma_f32_32x32x16_bf16 v[176:191], a[112:115], a[152:155], 0\n"
    "buffer_load_dwordx2 v[60:61], v96, s[48:51], s64 offen sc1\n"
    "v_exp_f32 v192, v192\n"
    "v_exp_f32 v193, v193\n"
    "v_exp_f32 v194, v194\n"
    "v_exp_f32 v195, v195\n"
    "v_cvt_pk_bf16_f32 v120, v192, v193\n"
    "v_mfma_f32_32x32x16_bf16 a[240:255], a[204:207], v[124:127], a[240:255]\n"
    "s_bfm_b64 s[80:81], 32, 0\n"
    "v_exp_f32 v196, v196\n"
    "v_exp_f32 v197, v197\n"
    "v_cvt_pk_bf16_f32 v121, v194, v195\n"
    "v_exp_f32 v198, v198\n"
    "v_exp_f32 v199, v199\n"
    "v_cvt_pk_bf16_f32 v122, v196, v197\n"
    "v_mfma_f32_32x32x16_bf16 v[176:191], a[116:119], a[156:159], v[176:191]\n"
    "global_load_dwordx4 a[188:191], v4, s[36:37] offset:-1024 sc1\n"
    "v_exp_f32 v200, v200\n"
    "v_exp_f32 v201, v201\n"
    "v_cvt_pk_bf16_f32 v123, v198, v199\n"
    "v_exp_f32 v202, v202\n"
    "v_exp_f32 v203, v203\n"
    "v_cvt_pk_bf16_f32 v124, v200, v201\n"
    "v_mfma_f32_32x32x16_bf16 v[160:175], a[200:203], v[120:123], v[160:175]\n"
    "global_load_lds_dwordx4 v89, s[40:41] offset:0\n"
    "v_exp_f32 v204, v204\n"
    "v_exp_f32 v205, v205\n"
    "v_cvt_pk_bf16_f32 v125, v202, v203\n"
    "v_exp_f32 v206, v206\n"
    "v_exp_f32 v207, v207\n"
    "v_cvt_pk_bf16_f32 v126, v204, v205\n"
    "v_cvt_pk_bf16_f32 v127, v206, v207\n"
    "v_mfma_f32_32x32x16_bf16 v[192:207], a[112:115], a[160:163], 0\n"
    "s_and_b32 s84, s84, 3\n"
    "v_exp_f32 v176, v176\n"
    "v_exp_f32 v177, v177\n"
    "v_exp_f32 v178, v178\n"
    "v_exp_f32 v179, v179\n"
    "v_cvt_pk_bf16_f32 v120, v176, v177\n"
    "v_mfma_f32_32x32x16_bf16 v[160:175], a[204:207], v[124:127], v[160:175]\n"
    "s_mul_i32 s84, s84, 12288\n"
    "v_exp_f32 v180, v180\n"
    "v_exp_f32 v181, v181\n"
    "v_cvt_pk_bf16_f32 v121, v178, v179\n"
    "v_exp_f32 v182, v182\n"
    "v_exp_f32 v183, v183\n"
    "v_cvt_pk_bf16_f32 v122, v180, v181\n"
    "v_mfma_f32_32x32x16_bf16 v[192:207], a[116:119], a[164:167], v[192:207]\n"
    "global_load_dwordx4 a[192:195], v4, s[36:37] offset:0 sc1\n"
    "v_exp_f32 v184, v184\n"
    "v_exp_f32 v185, v185\n"
    "v_cvt_pk_bf16_f32 v123, v182, v183\n"
    "v_exp_f32 v186, v186\n"
    "v_exp_f32 v187, v187\n"
    "v_cvt_pk_bf16_f32 v124, v184, v185\n"
    "v_mfma_f32_32x32x16_bf16 a[208:223], a[120:123], v[120:123], a[208:223]\n"
    "s_add_u32 s84, s84, 65536\n"
    "v_add_u32 v74, s84, v4\n"
    "v_exp_f32 v188, v188\n"
    "v_exp_f32 v189, v189\n"
    "v_cvt_pk_bf16_f32 v125, v186, v187\n"
    "v_exp_f32 v190, v190\n"
    "v_exp_f32 v191, v191\n"
    "v_cvt_pk_bf16_f32 v126, v188, v189\n"
    "v_cvt_pk_bf16_f32 v127, v190, v191\n"
    "v_mfma_f32_32x32x16_bf16 v[176:191], a[112:115], a[168:171], 0\n"
    "global_load_lds_dwordx4 v92, s[40:41] offset:1024\n"
    "v_exp_f32 v192, v192\n"
    "v_exp_f32 v193, v193\n"
    "v_exp_f32 v194, v194\n"
    "v_exp_f32 v195, v195\n"
    "v_cvt_pk_bf16_f32 v120, v192, v193\n"
    "v_mfma_f32_32x32x16_bf16 a[208:223], a[124:127], v[124:127], a[208:223]\n"
    "v_exp_f32 v196, v196\n"
    "v_exp_f32 v197, v197\n"
    "v_cvt_pk_bf16_f32 v121, v194, v195\n"
    "v_exp_f32 v198, v198\n"
    "v_exp_f32 v199, v199\n"
    "v_cvt_pk_bf16_f32 v122, v196, v197\n"
    "v_mfma_f32_32x32x16_bf16 v[176:191], a[116:119], a[172:175], v[176:191]\n"
    "global_load_dwordx4 a[196:199], v4, s[36:37] offset:1024 sc1\n"
    "v_exp_f32 v200, v200\n"
    "v_exp_f32 v201, v201\n"
    "v_cvt_pk_bf16_f32 v123, v198, v199\n"
    "v_exp_f32 v202, v202\n"
    "v_exp_f32 v203, v203\n"
    "v_cvt_pk_bf16_f32 v124, v200, v201\n"
    "v_mfma_f32_32x32x16_bf16 a[224:239], a[120:123], v[120:123], a[224:239]\n"
    "s_waitcnt vmcnt(5)\n"
    "v_cmp_ne_u64 vcc, s[78:79], v[60:61]\n"
    "v_exp_f32 v204, v204\n"
    "v_exp_f32 v205, v205\n"
    "v_cvt_pk_bf16_f32 v125, v202, v203\n"
    "v_exp_f32 v206, v206\n"
    "v_exp_f32 v207, v207\n"
    "v_cvt_pk_bf16_f32 v126, v204, v205\n"
    "v_cvt_pk_bf16_f32 v127, v206, v207\n"
    "v_mfma_f32_32x32x16_bf16 v[192:207], a[112:115], a[144:147], 0\n"
    "s_and_b64 vcc, vcc, s[80:81]\n"
    "s_cbranch_scc1 Lfhsl_%=\n"
    "Lfhok_%=:\n"
    "v_exp_f32 v176, v176\n"
    "v_exp_f32 v177, v177\n"
    "v_exp_f32 v178, v178\n"
    "v_exp_f32 v179, v179\n"
    "v_cvt_pk_bf16_f32 v120, v176, v177\n"
    "v_mfma_f32_32x32x16_bf16 a[224:239], a[124:127], v[124:127], a[224:239]\n"
    "global_load_dwordx4 a[0:3], v4, s[20:21] offset:0 sc1\n"
    "v_exp_f32 v180, v180\n"
    "v_exp_f32 v181, v181\n"
    "v_cvt_pk_bf16_f32 v121, v178, v179\n"
    "v_exp_f32 v182, v182\n"
    "v_exp_f32 v183, v183\n"
    "v_cvt_pk_bf16_f32 v122, v180, v181\n"
    "v_mfma_f32_32x32x16_bf16 v[192:207], a[116:119], a[148:151], v[192:207]\n"
    "global_load_dwordx4 a[200:203], v4, s[36:37] offset:2048 sc1\n"
    "v_exp_f32 v184, v184\n"
    "v_exp_f32 v185, v185\n"
    "v_cvt_pk_bf16_f32 v123, v182, v183\n"
    "v_exp_f32 v186, v186\n"
    "v_exp_f32 v187, v187\n"
    "v_cvt_pk_bf16_f32 v124, v184, v185\n"
    "v_mfma_f32_32x32x16_bf16 a[240:255], a[120:123], v[120:123], a[240:255]\n"
    "s_waitcnt vmcnt(13)\n"
    "v_exp_f32 v188, v188\n"
    "v_exp_f32 v189, v189\n"
    "v_cvt_pk_bf16_f32 v125, v186, v187\n"
    "v_exp_f32 v190, v190\n"
    "v_exp_f32 v191, v191\n"
    "v_cvt_pk_bf16_f32 v126, v188, v189\n"
    "v_cvt_pk_bf16_f32 v127, v190, v191\n"
    "v_mfma_f32_32x32x16_bf16 v[176:191], a[128:131], a[152:155], 0\n"
    "global_load_dwordx4 a[4:7], v4, s[20:21] offset:1024 sc1\n"
    "v_exp_f32 v192, v192\n"
    "v_exp_f32 v193, v193\n"
    "v_exp_f32 v194, v194\n"
    "v_exp_f32 v195, v195\n"
    "v_cvt_pk_bf16_f32 v120, v192, v193\n"
    "v_mfma_f32_32x32x16_bf16 a[240:255], a[124:127], v[124:127], a[240:255]\n"
    "global_load_dwordx4 a[8:11], v4, s[20:21] offset:2048 sc1\n"
    "v_exp_f32 v196, v196\n"
    "v_exp_f32 v197, v197\n"
    "v_cvt_pk_bf16_f32 v121, v194, v195\n"
    "v_exp_f32 v198, v198\n"
    "v_exp_f32 v199, v199\n"
    "v_cvt_pk_bf16_f32 v122, v196, v197\n"
    "v_mfma_f32_32x32x16_bf16 v[176:191], a[132:135], a[156:159], v[176:191]\n"
    "global_load_dwordx4 a[204:207], v4, s[36:37] offset:3072 sc1\n"
    "v_exp_f32 v200, v200\n"
    "v_exp_f32 v201, v201\n"
    "v_cvt_pk_bf16_f32 v123, v198, v199\n"
    "v_exp_f32 v202, v202\n"
    "v_exp_f32 v203, v203\n"
    "v_cvt_pk_bf16_f32 v124, v200, v201\n"
    "v_mfma_f32_32x32x16_bf16 v[160:175], a[120:123], v[120:123], v[160:175]\n"
    "global_load_dwordx4 a[12:15], v4, s[20:21] offset:3072 sc1\n"
    "v_exp_f32 v204, v204\n"
    "v_exp_f32 v205, v205\n"
    "v_cvt_pk_bf16_f32 v125, v202, v203\n"
    "v_exp_f32 v206, v206\n"
    "v_exp_f32 v207, v207\n"
    "v_cvt_pk_bf16_f32 v126, v204, v205\n"
    "v_cvt_pk_bf16_f32 v127, v206, v207\n"
    "v_mfma_f32_32x32x16_bf16 v[192:207], a[128:131], a[160:163], 0\n"
    "s_add_u32 s20, s20, 8192\n"
    "s_addc_u32 s21, s21, 0\n"
    "v_exp_f32 v176, v176\n"
    "v_exp_f32 v177, v177\n"
    "v_exp_f32 v178, v178\n"
    "v_exp_f32 v179, v179\n"
    "v_cvt_pk_bf16_f32 v120, v176, v177\n"
    "v_mfma_f32_32x32x16_bf16 v[160:175], a[124:127], v[124:127], v[160:175]\n"
    "global_load_dwordx4 a[16:19], v4, s[20:21] offset:0 sc1\n"
    "v_exp_f32 v180, v180\n"
    "v_exp_f32 v181, v181\n"
    "v_cvt_pk_bf16_f32 v121, v178, v179\n"
    "v_exp_f32 v182, v182\n"
    "v_exp_f32 v183, v183\n"
    "v_cvt_pk_bf16_f32 v122, v180, v181\n"
    "v_mfma_f32_32x32x16_bf16 v[192:207], a[132:135], a[164:167], v[192:207]\n"
    "global_load_lds_dwordx4 v93, s[40:41] offset:2048\n"
    "v_exp_f32 v184, v184\n"
    "v_exp_f32 v185, v185\n"
    "v_cvt_pk_bf16_f32 v123, v182, v183\n"
    "v_exp_f32 v186, v186\n"
    "v_exp_f32 v187, v187\n"
    "v_cvt_pk_bf16_f32 v124, v184, v185\n"
    "v_mfma_f32_32x32x16_bf16 a[208:223], a[136:139], v[120:123], a[208:223]\n"
    "global_load_dwordx4 a[20:23], v4, s[20:21] offset:1024 sc1\n"
    "v_exp_f32 v188, v188\n"
    "v_exp_f32 v189, v189\n"
    "v_cvt_pk_bf16_f32 v125, v186, v187\n"
    "v_exp_f32 v190, v190\n"
    "v_exp_f32 v191, v191\n"
    "v_cvt_pk_bf16_f32 v126, v188, v189\n"
    "v_cvt_pk_bf16_f32 v127, v190, v191\n"
    "v_mfma_f32_32x32x16_bf16 v[176:191], a[128:131], a[168:171], 0\n"
    "global_load_dwordx4 a[24:27], v4, s[20:21] offset:2048 sc1\n"
    "v_exp_f32 v192, v192\n"
    "v_exp_f32 v193, v193\n"
    "v_exp_f32 v194, v194\n"
    "v_exp_f32 v195, v195\n"
    "v_cvt_pk_bf16_f32 v120, v192, v193\n"
    "v_mfma_f32_32x32x16_bf16 a[208:223], a[140:143], v[124:127], a[208:223]\n"
    "v_exp_f32 v196, v196\n"
    "v_exp_f32 v197, v197\n"
    "v_cvt_pk_bf16_f32 v121, v194, v195\n"
    "v_exp_f32 v198, v198\n"
    "v_exp_f32 v199, v199\n"
    "v_cvt_pk_bf16_f32 v122, v196, v197\n"
    "v_mfma_f32_32x32x16_bf16 v[176:191], a[132:135], a[172:175], v[176:191]\n"
    "global_load_dwordx4 a[28:31], v4, s[20:21] offset:3072 sc1\n"
    "v_exp_f32 v200, v200\n"
    "v_exp_f32 v201, v201\n"
    "v_cvt_pk_bf16_f32 v123, v198, v199\n"
    "v_exp_f32 v202, v202\n"
    "v_exp_f32 v203, v203\n"
    "v_cvt_pk_bf16_f32 v124, v200, v201\n"
    "v_mfma_f32_32x32x16_bf16 a[224:239], a[136:139], v[120:123], a[224:239]\n"
    "s_add_u32 s20, s20, 8192\n"
    "s_addc_u32 s21, s21, 0\n"
    "v_exp_f32 v204, v204\n"
    "v_exp_f32 v205, v205\n"
    "v_cvt_pk_bf16_f32 v125, v202, v203\n"
    "v_exp_f32 v206, v206\n"
    "v_exp_f32 v207, v207\n"
    "v_cvt_pk_bf16_f32 v126, v204, v205\n"
    "v_cvt_pk_bf16_f32 v127, v206, v207\n"
    "v_mfma_f32_32x32x16_bf16 v[192:207], a[128:131], a[144:147], 0\n"
    "global_load_dwordx4 a[32:35], v4, s[20:21] offset:0 sc1\n"
    "v_exp_f32 v176, v176\n"
    "v_exp_f32 v177, v177\n"
    "v_exp_f32 v178, v178\n"
    "v_exp_f32 v179, v179\n"
    "v_cvt_pk_bf16_f32 v120, v176, v177\n"
    "v_mfma_f32_32x32x16_bf16 a[224:239], a[140:143], v[124:127], a[224:239]\n"
    "global_load_lds_dwordx4 v94, s[40:41] offset:3072\n"
    "v_exp_f32 v180, v180\n"
    "v_exp_f32 v181, v181\n"
    "v_cvt_pk_bf16_f32 v121, v178, v179\n"
    "v_exp_f32 v182, v182\n"
    "v_exp_f32 v183, v183\n"
    "v_cvt_pk_bf16_f32 v122, v180, v181\n"
    "v_mfma_f32_32x32x16_bf16 v[192:207], a[132:135], a[148:151], v[192:207]\n"
    "global_load_dwordx4 a[36:39], v4, s[20:21] offset:1024 sc1\n"
    "v_exp_f32 v184, v184\n"
    "v_exp_f32 v185, v185\n"
    "v_cvt_pk_bf16_f32 v123, v182, v183\n"
    "v_exp_f32 v186, v186\n"
    "v_exp_f32 v187, v187\n"
    "v_cvt_pk_bf16_f32 v124, v184, v185\n"
    "v_mfma_f32_32x32x16_bf16 a[240:255], a[136:139], v[120:123], a[240:255]\n"
    "s_waitcnt vmcnt(18)\n"
    "v_exp_f32 v188, v188\n"
    "v_exp_f32 v189, v189\n"
    "v_cvt_pk_bf16_f32 v125, v186, v187\n"
    "v_exp_f32 v190, v190\n"
    "v_exp_f32 v191, v191\n"
    "v_cvt_pk_bf16_f32 v126, v188, v189\n"
    "v_cvt_pk_bf16_f32 v127, v190, v191\n"
    "v_mfma_f32_32x32x16_bf16 v[176:191], a[176:179], a[152:155], 0\n"
    "global_load_dwordx4 a[40:43], v4, s[20:21] offset:2048 sc1\n"
    "v_exp_f32 v192, v192\n"
    "v_exp_f32 v193, v193\n"
    "v_exp_f32 v194, v194\n"
    "v_exp_f32 v195, v195\n"
    "v_cvt_pk_bf16_f32 v120, v192, v193\n"
    "v_mfma_f32_32x32x16_bf16 a[240:255], a[140:143], v[124:127], a[240:255]\n"
    "v_exp_f32 v196, v196\n"
    "v_exp_f32 v197, v197\n"
    "v_cvt_pk_bf16_f32 v121, v194, v195\n"
    "v_exp_f32 v198, v198\n"
    "v_exp_f32 v199, v199\n"
    "v_cvt_pk_bf16_f32 v122, v196, v197\n"
    "v_mfma_f32_32x32x16_bf16 v[176:191], a[180:183], a[156:159], v[176:191]\n"
    "global_load_dwordx4 a[44:47], v4, s[20:21] offset:3072 sc1\n"
    "v_exp_f32 v200, v200\n"
    "v_exp_f32 v201, v201\n"
    "v_cvt_pk_bf16_f32 v123, v198, v199\n"
    "v_exp_f32 v202, v202\n"
    "v_exp_f32 v203, v203\n"
    "v_cvt_pk_bf16_f32 v124, v200, v201\n"
    "v_mfma_f32_32x32x16_bf16 v[160:175], a[136:139], v[120:123], v[160:175]\n"
    "s_add_u32 s20, s20, 8192\n"
    "s_addc_u32 s21, s21, 0\n"
    "v_exp_f32 v204, v204\n"
    "v_exp_f32 v205, v205\n"
    "v_cvt_pk_bf16_f32 v125, v202, v203\n"
    "v_exp_f32 v206, v206\n"
    "v_exp_f32 v207, v207\n"
    "v_cvt_pk_bf16_f32 v126, v204, v205\n"
    "v_cvt_pk_bf16_f32 v127, v206, v207\n"
    "v_mfma_f32_32x32x16_bf16 v[192:207], a[176:179], a[160:163], 0\n"
    "global_load_dwordx4 a[48:51], v4, s[20:21] offset:0 sc1\n"
    "v_exp_f32 v176, v176\n"
    "v_exp_f32 v177, v177\n"
    "v_exp_f32 v178, v178\n"
    "v_exp_f32 v179, v179\n"
    "v_cvt_pk_bf16_f32 v120, v176, v177\n"
    "v_mfma_f32_32x32x16_bf16 v[160:175], a[140:143], v[124:127], v[160:175]\n"
    "s_add_u32 m0, m0, 4096\n"
    "v_exp_f32 v180, v180\n"
    "v_exp_f32 v181, v181\n"
    "v_cvt_pk_bf16_f32 v121, v178, v179\n"
    "v_exp_f32 v182, v182\n"
    "v_exp_f32 v183, v183\n"
    "v_cvt_pk_bf16_f32 v122, v180, v181\n"
    "v_mfma_f32_32x32x16_bf16 v[192:207], a[180:183], a[164:167], v[192:207]\n"
    "global_load_dwordx4 a[52:55], v4, s[20:21] offset:1024 sc1\n"
    "v_exp_f32 v184, v184\n"
    "v_exp_f32 v185, v185\n"
    "v_cvt_pk_bf16_f32 v123, v182, v183\n"
    "v_exp_f32 v186, v186\n"
    "v_exp_f32 v187, v187\n"
    "v_cvt_pk_bf16_f32 v124, v184, v185\n"
    "v_mfma_f32_32x32x16_bf16 a[208:223], a[184:187], v[120:123], a[208:223]\n"
    "global_load_dwordx4 a[56:59], v4, s[20:21] offset:2048 sc1\n"
    "v_exp_f32 v188, v188\n"
    "v_exp_f32 v189, v189\n"
    "v_cvt_pk_bf16_f32 v125, v186, v187\n"
    "v_exp_f32 v190, v190\n"
    "v_exp_f32 v191, v191\n"
    "v_cvt_pk_bf16_f32 v126, v188, v189\n"
    "v_cvt_pk_bf16_f32 v127, v190, v191\n"
    "v_mfma_f32_32x32x16_bf16 v[176:191], a[176:179], a[168:171], 0\n"
    "s_add_u32 s40, s40, 28672\n"
    "s_addc_u32 s41, s41, 0\n"
    "v_exp_f32 v192, v192\n"
    "v_exp_f32 v193, v193\n"
    "v_exp_f32 v194, v194\n"
    "v_exp_f32 v195, v195\n"
    "v_cvt_pk_bf16_f32 v120, v192, v193\n"
    "v_mfma_f32_32x32x16_bf16 a[208:223], a[188:191], v[124:127], a[208:223]\n"
    "global_load_dwordx4 a[60:63], v4, s[20:21] offset:3072 sc1\n"
    "v_exp_f32 v196, v196\n"
    "v_exp_f32 v197, v197\n"
    "v_cvt_pk_bf16_f32 v121, v194, v195\n"
    "v_exp_f32 v198, v198\n"
    "v_exp_f32 v199, v199\n"
    "v_cvt_pk_bf16_f32 v122, v196, v197\n"
    "v_mfma_f32_32x32x16_bf16 v[176:191], a[180:183], a[172:175], v[176:191]\n"
    "s_add_u32 s20, s20, 8192\n"
    "s_addc_u32 s21, s21, 0\n"
    "v_exp_f32 v200, v200\n"
    "v_exp_f32 v201, v201\n"
    "v_cvt_pk_bf16_f32 v123, v198, v199\n"
    "v_exp_f32 v202, v202\n"
    "v_exp_f32 v203, v203\n"
    "v_cvt_pk_bf16_f32 v124, v200, v201\n"
    "v_mfma_f32_32x32x16_bf16 a[224:239], a[184:187], v[120:123], a[224:239]\n"
    "global_load_dwordx4 a[64:67], v4, s[20:21] offset:0 sc1\n"
    "v_exp_f32 v204, v204\n"
    "v_exp_f32 v205, v205\n"
    "v_cvt_pk_bf16_f32 v125, v202, v203\n"
    "v_exp_f32 v206, v206\n"
    "v_exp_f32 v207, v207\n"
    "v_cvt_pk_bf16_f32 v126, v204, v205\n"
    "v_cvt_pk_bf16_f32 v127, v206, v207\n"
    "v_mfma_f32_32x32x16_bf16 v[192:207], a[176:179], a[144:147], 0\n"
    "global_load_lds_dwordx4 v89, s[40:41] offset:0\n"
    "v_exp_f32 v176, v176\n"
    "v_exp_f32 v177, v177\n"
    "v_exp_f32 v178, v178\n"
    "v_exp_f32 v179, v179\n"
    "v_cvt_pk_bf16_f32 v120, v176, v177\n"
    "v_mfma_f32_32x32x16_bf16 a[224:239], a[188:191], v[124:127], a[224:239]\n"
    "global_load_dwordx4 a[68:71], v4, s[20:21] offset:1024 sc1\n"
    "v_exp_f32 v180, v180\n"
    "v_exp_f32 v181, v181\n"
    "v_cvt_pk_bf16_f32 v121, v178, v179\n"
    "v_exp_f32 v182, v182\n"
    "v_exp_f32 v183, v183\n"
    "v_cvt_pk_bf16_f32 v122, v180, v181\n"
    "v_mfma_f32_32x32x16_bf16 v[192:207], a[180:183], a[148:151], v[192:207]\n"
    "global_load_dwordx4 a[72:75], v4, s[20:21] offset:2048 sc1\n"
    "v_exp_f32 v184, v184\n"
    "v_exp_f32 v185, v185\n"
    "v_cvt_pk_bf16_f32 v123, v182, v183\n"
    "v_exp_f32 v186, v186\n"
    "v_exp_f32 v187, v187\n"
    "v_cvt_pk_bf16_f32 v124, v184, v185\n"
    "v_mfma_f32_32x32x16_bf16 a[240:255], a[184:187], v[120:123], a[240:255]\n"
    "s_waitcnt vmcnt(19)\n"
    "v_exp_f32 v188, v188\n"
    "v_exp_f32 v189, v189\n"
    "v_cvt_pk_bf16_f32 v125, v186, v187\n"
    "v_exp_f32 v190, v190\n"
    "v_exp_f32 v191, v191\n"
    "v_cvt_pk_bf16_f32 v126, v188, v189\n"
    "v_cvt_pk_bf16_f32 v127, v190, v191\n"
    "v_mfma_f32_32x32x16_bf16 v[176:191], a[192:195], a[152:155], 0\n"
    "global_load_dwordx4 a[76:79], v4, s[20:21] offset:3072 sc1\n"
    "v_exp_f32 v192, v192\n"
    "v_exp_f32 v193, v193\n"
    "v_exp_f32 v194, v194\n"
    "v_exp_f32 v195, v195\n"
    "v_cvt_pk_bf16_f32 v120, v192, v193\n"
    "v_mfma_f32_32x32x16_bf16 a[240:255], a[188:191], v[124:127], a[240:255]\n"
    "s_add_u32 s20, s20, 8192\n"
    "s_addc_u32 s21, s21, 0\n"
    "v_exp_f32 v196, v196\n"
    "v_exp_f32 v197, v197\n"
    "v_cvt_pk_bf16_f32 v121, v194, v195\n"
    "v_exp_f32 v198, v198\n"
    "v_exp_f32 v199, v199\n"
    "v_cvt_pk_bf16_f32 v122, v196, v197\n"
    "v_mfma_f32_32x32x16_bf16 v[176:191], a[196:199], a[156:159], v[176:191]\n"
    "global_load_dwordx4 a[80:83], v4, s[20:21] offset:0 sc1\n"
    "v_exp_f32 v200, v200\n"
    "v_exp_f32 v201, v201\n"
    "v_cvt_pk_bf16_f32 v123, v198, v199\n"
    "v_exp_f32 v202, v202\n"
    "v_exp_f32 v203, v203\n"
    "v_cvt_pk_bf16_f32 v124, v200, v201\n"
    "v_mfma_f32_32x32x16_bf16 v[160:175], a[184:187], v[120:123], v[160:175]\n"
    "global_load_lds_dwordx4 v92, s[40:41] offset:1024\n"
    "v_exp_f32 v204, v204\n"
    "v_exp_f32 v205, v205\n"
    "v_cvt_pk_bf16_f32 v125, v202, v203\n"
    "v_exp_f32 v206, v206\n"
    "v_exp_f32 v207, v207\n"
    "v_cvt_pk_bf16_f32 v126, v204, v205\n"
    "v_cvt_pk_bf16_f32 v127, v206, v207\n"
    "v_mfma_f32_32x32x16_bf16 v[192:207], a[192:195], a[160:163], 0\n"
    "global_load_dwordx4 a[84:87], v4, s[20:21] offset:1024 sc1\n"
    "v_exp_f32 v176, v176\n"
    "v_exp_f32 v177, v177\n"
    "v_exp_f32 v178, v178\n"
    "v_exp_f32 v179, v179\n"
    "v_cvt_pk_bf16_f32 v120, v176, v177\n"
    "v_mfma_f32_32x32x16_bf16 v[160:175], a[188:191], v[124:127], v[160:175]\n"
    "global_load_dwordx4 a[88:91], v4, s[20:21] offset:2048 sc1\n"
    "v_exp_f32 v180, v180\n"
    "v_exp_f32 v181, v181\n"
    "v_cvt_pk_bf16_f32 v121, v178, v179\n"
    "v_exp_f32 v182, v182\n"
    "v_exp_f32 v183, v183\n"
    "v_cvt_pk_bf16_f32 v122, v180, v181\n"
    "v_mfma_f32_32x32x16_bf16 v[192:207], a[196:199], a[164:167], v[192:207]\n"
    "v_exp_f32 v184, v184\n"
    "v_exp_f32 v185, v185\n"
    "v_cvt_pk_bf16_f32 v123, v182, v183\n"
    "v_exp_f32 v186, v186\n"
    "v_exp_f32 v187, v187\n"
    "v_cvt_pk_bf16_f32 v124, v184, v185\n"
    "v_mfma_f32_32x32x16_bf16 a[208:223], a[200:203], v[120:123], a[208:223]\n"
    "global_load_dwordx4 a[92:95], v4, s[20:21] offset:3072 sc1\n"
    "v_exp_f32 v188, v188\n"
    "v_exp_f32 v189, v189\n"
    "v_cvt_pk_bf16_f32 v125, v186, v187\n"
    "v_exp_f32 v190, v190\n"
    "v_exp_f32 v191, v191\n"
    "v_cvt_pk_bf16_f32 v126, v188, v189\n"
    "v_cvt_pk_bf16_f32 v127, v190, v191\n"
    "v_mfma_f32_32x32x16_bf16 v[176:191], a[192:195], a[168:171], 0\n"
    "s_add_u32 s20, s20, 8192\n"
    "s_addc_u32 s21, s21, 0\n"
    "v_exp_f32 v192, v192\n"
    "v_exp_f32 v193, v193\n"
    "v_exp_f32 v194, v194\n"
    "v_exp_f32 v195, v195\n"
    "v_cvt_pk_bf16_f32 v120, v192, v193\n"
    "v_mfma_f32_32x32x16_bf16 a[208:223], a[204:207], v[124:127], a[208:223]\n"
    "global_load_dwordx4 a[96:99], v4, s[20:21] offset:0 sc1\n"
    "v_exp_f32 v196, v196\n"
    "v_exp_f32 v197, v197\n"
    "v_cvt_pk_bf16_f32 v121, v194, v195\n"
    "v_exp_f32 v198, v198\n"
    "v_exp_f32 v199, v199\n"
    "v_cvt_pk_bf16_f32 v122, v196, v197\n"
    "v_mfma_f32_32x32x16_bf16 v[176:191], a[196:199], a[172:175], v[176:191]\n"
    "v_exp_f32 v200, v200\n"
    "v_exp_f32 v201, v201\n"
    "v_cvt_pk_bf16_f32 v123, v198, v199\n"
    "v_exp_f32 v202, v202\n"
    "v_exp_f32 v203, v203\n"
    "v_cvt_pk_bf16_f32 v124, v200, v201\n"
    "v_mfma_f32_32x32x16_bf16 a[224:239], a[200:203], v[120:123], a[224:239]\n"
    "ds_write_b128 v72, a[208:211] offset:0\n"
    "v_exp_f32 v204, v204\n"
    "v_exp_f32 v205, v205\n"
    "v_cvt_pk_bf16_f32 v125, v202, v203\n"
    "v_exp_f32 v206, v206\n"
    "v_exp_f32 v207, v207\n"
    "v_cvt_pk_bf16_f32 v126, v204, v205\n"
    "v_cvt_pk_bf16_f32 v127, v206, v207\n"
    "v_mfma_f32_32x32x16_bf16 v[192:207], a[192:195], a[144:147], 0\n"
    "ds_write_b128 v72, a[212:215] offset:1024\n"
    "v_exp_f32 v176, v176\n"
    "v_exp_f32 v177, v177\n"
    "v_exp_f32 v178, v178\n"
    "v_exp_f32 v179, v179\n"
    "v_cvt_pk_bf16_f32 v120, v176, v177\n"
    "v_mfma_f32_32x32x16_bf16 a[224:239], a[204:207], v[124:127], a[224:239]\n"
    "ds_write_b128 v72, a[216:219] offset:2048\n"
    "v_exp_f32 v180, v180\n"
    "v_exp_f32 v181, v181\n"
    "v_cvt_pk_bf16_f32 v121, v178, v179\n"
    "v_exp_f32 v182, v182\n"
    "v_exp_f32 v183, v183\n"
    "v_cvt_pk_bf16_f32 v122, v180, v181\n"
    "v_mfma_f32_32x32x16_bf16 v[192:207], a[196:199], a[148:151], v[192:207]\n"
    "ds_write_b128 v72, a[220:223] offset:3072\n"
    "v_exp_f32 v184, v184\n"
    "v_exp_f32 v185, v185\n"
    "v_cvt_pk_bf16_f32 v123, v182, v183\n"
    "v_exp_f32 v186, v186\n"
    "v_exp_f32 v187, v187\n"
    "v_cvt_pk_bf16_f32 v124, v184, v185\n"
    "v_mfma_f32_32x32x16_bf16 a[240:255], a[200:203], v[120:123], a[240:255]\n"
    "ds_write_b128 v73, a[224:227] offset:0\n"
    "v_exp_f32 v188, v188\n"
    "v_exp_f32 v189, v189\n"
    "v_cvt_pk_bf16_f32 v125, v186, v187\n"
    "v_exp_f32 v190, v190\n"
    "v_exp_f32 v191, v191\n"
    "v_cvt_pk_bf16_f32 v126, v188, v189\n"
    "v_cvt_pk_bf16_f32 v127, v190, v191\n"
    "s_nop 0\n"
    "ds_write_b128 v73, a[228:231] offset:1024\n"
    "v_exp_f32 v192, v192\n"
    "v_exp_f32 v193, v193\n"
    "v_exp_f32 v194, v194\n"
    "v_exp_f32 v195, v195\n"
    "v_cvt_pk_bf16_f32 v120, v192, v193\n"
    "v_mfma_f32_32x32x16_bf16 a[240:255], a[204:207], v[124:127], a[240:255]\n"
    "ds_write_b128 v73, a[232:235] offset:2048\n"
    "v_exp_f32 v196, v196\n"
    "v_exp_f32 v197, v197\n"
    "v_cvt_pk_bf16_f32 v121, v194, v195\n"
    "v_exp_f32 v198, v198\n"
    "v_exp_f32 v199, v199\n"
    "v_cvt_pk_bf16_f32 v122, v196, v197\n"
    "s_nop 0\n"
    "ds_write_b128 v73, a[236:239] offset:3072\n"
    "v_exp_f32 v200, v200\n"
    "v_exp_f32 v201, v201\n"
    "v_cvt_pk_bf16_f32 v123, v198, v199\n"
    "v_exp_f32 v202, v202\n"
    "v_exp_f32 v203, v203\n"
    "v_cvt_pk_bf16_f32 v124, v200, v201\n"
    "v_mfma_f32_32x32x16_bf16 v[160:175], a[200:203], v[120:123], v[160:175]\n"
    "ds_write_b128 v74, a[240:243] offset:0\n"
    "v_exp_f32 v204, v204\n"
    "v_exp_f32 v205, v205\n"
    "v_cvt_pk_bf16_f32 v125, v202, v203\n"
    "v_exp_f32 v206, v206\n"
    "v_exp_f32 v207, v207\n"
    "v_cvt_pk_bf16_f32 v126, v204, v205\n"
    "v_cvt_pk_bf16_f32 v127, v206, v207\n"
    "ds_write_b128 v74, a[244:247] offset:1024\n"
    "s_nop 0\n"
    "v_mfma_f32_32x32x16_bf16 v[160:175], a[204:207], v[124:127], v[160:175]\n"
    "ds_write_b128 v74, a[248:251] offset:2048\n"
    "ds_write_b128 v74, a[252:255] offset:3072\n"
    "global_load_dwordx4 a[100:103], v4, s[20:21] offset:1024 sc1\n"
    "global_load_dwordx4 a[104:107], v4, s[20:21] offset:2048 sc1\n"
    "global_load_dwordx4 a[108:111], v4, s[20:21] offset:3072 sc1\n"
    "s_waitcnt vmcnt(7)\n"
    "s_waitcnt lgkmcnt(0)\n"
    "s_barrier\n"
    "ds_read_b128 v[220:223], v71 offset:3072\n"
    "ds_read_b128 v[236:239], v71 offset:7168\n"
    "ds_read_b128 v[252:255], v71 offset:11264\n"
    "ds_read_b128 v[208:211], v71 offset:0\n"
    "ds_read_b128 v[224:227], v71 offset:4096\n"
    "ds_read_b128 v[240:243], v71 offset:8192\n"
    "ds_read_b128 v[212:215], v71 offset:1024\n"
    "ds_read_b128 v[228:231], v71 offset:5120\n"
    "ds_read_b128 v[244:247], v71 offset:9216\n"
    "ds_read_b128 v[216:219], v71 offset:2048\n"
    "ds_read_b128 v[232:235], v71 offset:6144\n"
    "ds_read_b128 v[248:251], v71 offset:10240\n"
    "s_mul_i32 s84, s73, 28544\n"
    "s_add_u32 s84, s84, 512\n"
    "s_add_u32 s34, s32, s84\n"
    "s_addc_u32 s35, s33, 0\n"
    "s_add_u32 s32, s32, 256\n"
    "s_addc_u32 s33, s33, 0\n"
    "s_mul_i32 s84, s73, -10240\n"
    "s_add_u32 s30, s63, s84\n"
    "s_add_u32 m0, s30, 57344\n"
    "s_add_u32 s63, s63, 28672\n"
    "s_cmp_eq_u32 s73, 0\n"
    "s_lshl_b32 s84, s74, 2\n"
    "v_xor_b32 v9, s84, v45\n"
    "v_lshlrev_b32 v9, 4, v9\n"
    "v_lshl_or_b32 v9, v2, 7, v9\n"
    "v_mul_u32_u24 v10, 7168, v3\n"
    "s_lshl_b32 s84, s74, 12\n"
    "s_xor_b32 s85, s84, 4096\n"
    "v_xor_b32 v11, 0, v9\n"
    "v_add3_u32 v14, v11, v10, s84\n"
    "v_add3_u32 v24, v11, v10, s85\n"
    "v_xor_b32 v11, 16, v9\n"
    "v_add3_u32 v16, v11, v10, s84\n"
    "v_add3_u32 v25, v11, v10, s85\n"
    "v_xor_b32 v11, 32, v9\n"
    "v_add3_u32 v17, v11, v10, s84\n"
    "v_add3_u32 v26, v11, v10, s85\n"
    "v_xor_b32 v11, 48, v9\n"
    "v_add3_u32 v20, v11, v10, s84\n"
    "v_add3_u32 v27, v11, v10, s85\n"
    "s_waitcnt lgkmcnt(11)\n"
    "v_pk_add_f32 v[172:173], v[172:173], v[220:221]\n"
    "v_pk_add_f32 v[174:175], v[174:175], v[222:223]\n"
    "s_waitcnt lgkmcnt(10)\n"
    "v_pk_add_f32 v[172:173], v[172:173], v[236:237]\n"
    "v_pk_add_f32 v[174:175], v[174:175], v[238:239]\n"
    "s_waitcnt lgkmcnt(9)\n"
    "v_pk_add_f32 v[172:173], v[172:173], v[252:253]\n"
    "v_pk_add_f32 v[174:175], v[174:175], v[254:255]\n"
    "v_mov_b32 v18, v172\n"
    "v_mov_b32 v5, v172\n"
    "v_pk_mul_f32 v[116:117], v[172:173], 0\n"
    "v_pk_fma_f32 v[116:117], v[174:175], 0, v[116:117]\n"
    "v_permlane32_swap_b32 v18, v5\n"
    "s_waitcnt lgkmcnt(8)\n"
    "v_pk_add_f32 v[160:161], v[160:161], v[208:209]\n"
    "v_pk_add_f32 v[162:163], v[162:163], v[210:211]\n"
    "v_rcp_f32 v18, v5\n"
    "v_mov_b32 v8, 1602224128\n"
    "v_cmp_nle_f32 vcc, 864026624, v5\n"
    "v_cmp_nge_f32 s[80:81], v8, v5\n"
    "v_mul_f32 v18, v18, v15\n"
    "s_or_b64 s[80:81], s[80:81], vcc\n"
    "s_cmp_eq_u32 s73, 0\n"
    "s_waitcnt lgkmcnt(7)\n"
    "v_pk_add_f32 v[160:161], v[160:161], v[224:225]\n"
    "v_pk_add_f32 v[162:163], v[162:163], v[226:227]\n"
    "s_waitcnt lgkmcnt(6)\n"
    "v_pk_add_f32 v[160:161], v[160:161], v[240:241]\n"
    "v_pk_add_f32 v[162:163], v[162:163], v[242:243]\n"
    "v_pk_fma_f32 v[116:117], v[160:161], 0, v[116:117]\n"
    "v_pk_fma_f32 v[116:117], v[162:163], 0, v[116:117]\n"
    "s_waitcnt lgkmcnt(5)\n"
    "v_pk_add_f32 v[164:165], v[164:165], v[212:213]\n"
    "v_pk_add_f32 v[166:167], v[166:167], v[214:215]\n"
    "s_waitcnt lgkmcnt(4)\n"
    "v_pk_add_f32 v[164:165], v[164:165], v[228:229]\n"
    "v_pk_add_f32 v[166:167], v[166:167], v[230:231]\n"
    "s_waitcnt lgkmcnt(3)\n"
    "v_pk_add_f32 v[164:165], v[164:165], v[244:245]\n"
    "v_pk_add_f32 v[166:167], v[166:167], v[246:247]\n"
    "v_pk_fma_f32 v[116:117], v[164:165], 0, v[116:117]\n"
    "v_pk_fma_f32 v[116:117], v[166:167], 0, v[116:117]\n"
    "s_waitcnt lgkmcnt(2)\n"
    "v_pk_add_f32 v[168:169], v[168:169], v[216:217]\n"
    "v_pk_add_f32 v[170:171], v[170:171], v[218:219]\n"
    "s_waitcnt lgkmcnt(1)\n"
    "v_pk_add_f32 v[168:169], v[168:169], v[232:233]\n"
    "v_pk_add_f32 v[170:171], v[170:171], v[234:235]\n"
    "s_waitcnt lgkmcnt(0)\n"
    "s_barrier\n"
    "s_cbranch_scc0 Lc6_%=\n"
    "global_load_lds_dwordx4 v78, s[34:35] offset:3072\n"
    "Lc6_%=:\n"
    "v_pk_add_f32 v[168:169], v[168:169], v[248:249]\n"
    "v_pk_add_f32 v[170:171], v[170:171], v[250:251]\n"
    "v_pk_fma_f32 v[116:117], v[168:169], 0, v[116:117]\n"
    "v_pk_fma_f32 v[116:117], v[170:171], 0, v[116:117]\n"
    "v_cmp_u_f32 vcc, v116, v117\n"
    "s_nop 0\n"
    "s_or_b64 vcc, vcc, s[80:81]\n"
    "s_cbranch_scc1 Lfb_%=\n"
    "Lnorm2_%=:\n"
    "v_cmp_eq_u32 vcc, 1, v3\n"
    "v_pk_mul_f32 v[160:161], v[18:19], v[160:161] op_sel_hi:[0,1]\n"
    "v_pk_mul_f32 v[162:163], v[18:19], v[162:163] op_sel_hi:[0,1]\n"
    "v_cvt_pk_bf16_f32 v120, v160, v161\n"
    "v_pk_mul_f32 v[164:165], v[18:19], v[164:165] op_sel_hi:[0,1]\n"
    "v_cvt_pk_bf16_f32 v121, v162, v163\n"
    "v_pk_mul_f32 v[166:167], v[18:19], v[166:167] op_sel_hi:[0,1]\n"
    "v_cvt_pk_bf16_f32 v122, v164, v165\n"
    "v_pk_mul_f32 v[168:169], v[18:19], v[168:169] op_sel_hi:[0,1]\n"
    "v_cvt_pk_bf16_f32 v123, v166, v167\n"
    "v_pk_mul_f32 v[170:171], v[18:19], v[170:171] op_sel_hi:[0,1]\n"
    "v_cvt_pk_bf16_f32 v124, v168, v169\n"
    "v_pk_mul_f32 v[172:173], v[18:19], v[172:173] op_sel_hi:[0,1]\n"
    "v_cvt_pk_bf16_f32 v125, v170, v171\n"
    "v_pk_mul_f32 v[174:175], v[18:19], v[174:175] op_sel_hi:[0,1]\n"
    "v_cvt_pk_bf16_f32 v126, v172, v173\n"
    "buffer_store_dwordx4 v[120:123], v4, s[48:51], s57 offen sc1\n"
    "v_cvt_pk_bf16_f32 v127, v174, v175\n"
    "v_cndmask_b32 v126, v126, 0, vcc\n"
    "v_cndmask_b32 v127, v127, 0, vcc\n"
    "buffer_store_dwordx4 v[124:127], v4, s[48:51], s57 offen offset:1024 sc1\n"
    "ds_read_b128 v[208:211], v14 offset:0\n"
    "ds_read_b128 v[212:215], v24 offset:0\n"
    "ds_read_b128 v[216:219], v16 offset:0\n"
    "ds_read_b128 v[220:223], v25 offset:0\n"
    "ds_read_b128 v[224:227], v17 offset:0\n"
    "ds_read_b128 v[228:231], v26 offset:0\n"
    "ds_read_b128 v[232:235], v20 offset:0\n"
    "ds_read_b128 v[236:239], v27 offset:0\n"
    "s_waitcnt vmcnt(30)\n"
    "s_waitcnt lgkmcnt(6)\n"
    "v_mfma_f32_32x32x16_bf16 v[176:191], v[208:211], a[0:3], 0\n"
    "ds_read_b128 v[240:243], v14 offset:14336\n"
    "global_load_lds_dwordx4 v75, s[34:35] offset:0\n"
    "s_add_u32 s85, s59, 4096\n"
    "s_add_u32 s40, s48, s85\n"
    "s_addc_u32 s41, s49, 0\n"
    "v_mfma_f32_32x32x16_bf16 v[192:207], v[212:215], a[0:3], 0\n"
    "ds_read_b128 v[244:247], v24 offset:14336\n"
    "global_load_lds_dwordx4 v76, s[34:35] offset:1024\n"
    "s_add_u32 s85, s59, 12288\n"
    "s_add_u32 s42, s48, s85\n"
    "s_addc_u32 s43, s49, 0\n"
    "s_waitcnt lgkmcnt(6)\n"
    "v_mfma_f32_32x32x16_bf16 v[176:191], v[216:219], a[4:7], v[176:191]\n"
    "ds_read_b128 v[248:251], v16 offset:14336\n"
    "global_load_lds_dwordx4 v77, s[34:35] offset:2048\n"
    "s_mov_b32 m0, s63\n"
    "s_xor_b32 s84, s76, 31\n"
    "s_or_b32 s84, s84, s72\n"
    "s_or_b32 s84, s84, s75\n"
    "v_mfma_f32_32x32x16_bf16 v[192:207], v[220:223], a[4:7], v[192:207]\n"
    "ds_read_b128 v[252:255], v25 offset:14336\n"
    "global_load_lds_dwordx4 v75, s[32:33] offset:0\n"
    "s_cmp_eq_u32 s84, 0\n"
    "s_cselect_b32 s84, 0, 2147483648\n"
    "s_waitcnt lgkmcnt(6)\n"
    "v_mfma_f32_32x32x16_bf16 v[176:191], v[224:227], a[8:11], v[176:191]\n"
    "ds_read_b128 v[208:211], v17 offset:14336\n"
    "global_load_lds_dwordx4 v76, s[32:33] offset:1024\n"
    "v_and_b32 v57, 896, v57\n"
    "v_mfma_f32_32x32x16_bf16 v[192:207], v[228:231], a[8:11], v[192:207]\n"
    "ds_read_b128 v[212:215], v26 offset:14336\n"
    "global_load_lds_dwordx4 v77, s[32:33] offset:2048\n"
    "v_lshlrev_b32 v72, 4, v57\n"
    "s_waitcnt lgkmcnt(6)\n"
    "v_mfma_f32_32x32x16_bf16 v[176:191], v[232:235], a[12:15], v[176:191]\n"
    "ds_read_b128 v[216:219], v20 offset:14336\n"
    "global_load_lds_dwordx4 v78, s[32:33] offset:3072\n"
    "s_add_u32 m0, m0, 4096\n"
    "v_or_b32 v73, s84, v52\n"
    "v_mfma_f32_32x32x16_bf16 v[192:207], v[236:239], a[12:15], v[192:207]\n"
    "ds_read_b128 v[220:223], v27 offset:14336\n"
    "global_load_lds_dwordx4 v79, s[32:33] offset:0\n"
    "v_sub_f32 v116, 1.0, v15\n"
    "s_waitcnt vmcnt(33)\n"
    "s_waitcnt lgkmcnt(6)\n"
    "v_mfma_f32_32x32x16_bf16 v[176:191], v[240:243], a[16:19], v[176:191]\n"
    "ds_read_b128 v[224:227], v14 offset:28672\n"
    "global_load_lds_dwordx4 v80, s[32:33] offset:1024\n"
    "v_lshrrev_b32 v5, 1, v21\n"
    "v_mfma_f32_32x32x16_bf16 v[192:207], v[244:247], a[16:19], v[192:207]\n"
    "ds_read_b128 v[228:231], v24 offset:28672\n"
    "global_load_lds_dwordx4 v81, s[32:33] offset:2048\n"
    "v_xor_b32 v5, s74, v5\n"
    "s_waitcnt lgkmcnt(6)\n"
    "v_mfma_f32_32x32x16_bf16 v[176:191], v[248:251], a[20:23], v[176:191]\n"
    "ds_read_b128 v[232:235], v16 offset:28672\n"
    "v_lshlrev_b32 v5, 6, v5\n"
    "v_lshl_or_b32 v5, v23, 5, v5\n"
    "v_lshl_or_b32 v5, v22, 3, v5\n"
    "v_lshl_or_b32 v5, v21, 7, v5\n"
    "v_mfma_f32_32x32x16_bf16 v[192:207], v[252:255], a[20:23], v[192:207]\n"
    "ds_read_b128 v[236:239], v25 offset:28672\n"
    "s_mul_i32 s84, s74, 28672\n"
    "s_add_u32 s84, s84, 106496\n"
    "v_add_u32 v92, s84, v5\n"
    "v_lshl_add_u32 v89, v3, 9, v92\n"
    "s_waitcnt lgkmcnt(6)\n"
    "v_mfma_f32_32x32x16_bf16 v[176:191], v[208:211], a[24:27], v[176:191]\n"
    "ds_read_b128 v[240:243], v17 offset:28672\n"
    "v_xor_b32 v93, 64, v89\n"
    "v_xor_b32 v94, 64, v92\n"
    "s_lshl1_add_u32 s84, s74, s73\n"
    "s_lshl_b32 s84, s84, 13\n"
    "v_mfma_f32_32x32x16_bf16 v[192:207], v[212:215], a[24:27], v[192:207]\n"
    "ds_read_b128 v[244:247], v26 offset:28672\n"
    "s_lshl_b32 s85, s74, 7\n"
    "s_add_u32 s84, s84, s85\n"
    "v_and_b32 v5, 7, v1\n"
    "v_xor_b32 v5, v3, v5\n"
    "s_waitcnt lgkmcnt(6)\n"
    "v_mfma_f32_32x32x16_bf16 v[176:191], v[216:219], a[28:31], v[176:191]\n"
    "ds_read_b128 v[248:251], v20 offset:28672\n"
    "v_lshlrev_b32 v5, 4, v5\n"
    "v_lshl_or_b32 v5, v2, 8, v5\n"
    "v_add_u32 v65, s84, v5\n"
    "v_lshrrev_b32 v5, 4, v1\n"
    "v_mfma_f32_32x32x16_bf16 v[192:207], v[220:223], a[28:31], v[192:207]\n"
    "ds_read_b128 v[252:255], v27 offset:28672\n"
    "v_and_b32 v6, 15, v1\n"
    "v_xor_b32 v7, v5, v6\n"
    "v_lshlrev_b32 v7, 4, v7\n"
    "v_lshl_or_b32 v7, v5, 8, v7\n"
    "s_waitcnt vmcnt(12)\n"
    "s_waitcnt lgkmcnt(6)\n"
    "v_mfma_f32_32x32x16_bf16 v[176:191], v[224:227], a[32:35], v[176:191]\n"
    "ds_read_b128 v[208:211], v14 offset:43008\n"
    "s_lshl_b32 s84, s72, 12\n"
    "v_add_u32 v74, s84, v7\n"
    "v_xor_b32 v64, 64, v74\n"
    "v_lshl_or_b32 v7, v23, 3, v3\n"
    "v_mfma_f32_32x32x16_bf16 v[192:207], v[228:231], a[32:35], v[192:207]\n"
    "ds_read_b128 v[212:215], v24 offset:43008\n"
    "s_lshl3_add_u32 s84, s74, s73\n"
    "s_lshl_b32 s84, s84, 2\n"
    "v_add_u32 v7, s84, v7\n"
    "v_mul_u32_u24 v7, 1792, v7\n"
    "s_waitcnt lgkmcnt(6)\n"
    "v_mfma_f32_32x32x16_bf16 v[176:191], v[232:235], a[36:39], v[176:191]\n"
    "ds_read_b128 v[216:219], v16 offset:43008\n"
    "v_cmp_lt_u32 vcc, 13, v6\n"
    "v_lshl_add_u32 v7, v6, 4, v7\n"
    "v_mov_b32 v5, 2147483648\n"
    "v_cndmask_b32 v69, v7, v5, vcc\n"
    "v_mfma_f32_32x32x16_bf16 v[192:207], v[236:239], a[36:39], v[192:207]\n"
    "ds_read_b128 v[220:223], v25 offset:43008\n"
    "s_mul_i32 s84, s75, 112\n"
    "s_add_u32 s39, s84, s92\n"
    "s_lshl_b32 s39, s39, 1\n"
    "s_add_u32 s44, s39, 28672\n"
    "s_waitcnt lgkmcnt(6)\n"
    "v_mfma_f32_32x32x16_bf16 v[176:191], v[240:243], a[40:43], v[176:191]\n"
    "ds_read_b128 v[224:227], v17 offset:43008\n"
    "v_permlane32_swap_b32 v144, v146\n"
    "v_permlane32_swap_b32 v145, v147\n"
    "v_permlane32_swap_b32 v148, v150\n"
    "v_permlane32_swap_b32 v149, v151\n"
    "v_mfma_f32_32x32x16_bf16 v[192:207], v[244:247], a[40:43], v[192:207]\n"
    "ds_read_b128 v[228:231], v26 offset:43008\n"
    "v_lshlrev_b32 v5, 16, v144\n"
    "v_lshlrev_b32 v6, 16, v152\n"
    "v_and_b32 v7, 0xffff0000, v144\n"
    "v_and_b32 v8, 0xffff0000, v152\n"
    "s_waitcnt lgkmcnt(6)\n"
    "v_mfma_f32_32x32x16_bf16 v[176:191], v[248:251], a[44:47], v[176:191]\n"
    "ds_read_b128 v[232:235], v20 offset:43008\n"
    "v_mul_f32 v160, v5, v6\n"
    "v_mul_f32 v161, v7, v8\n"
    "v_lshlrev_b32 v5, 16, v145\n"
    "v_lshlrev_b32 v6, 16, v153\n"
    "v_mfma_f32_32x32x16_bf16 v[192:207], v[252:255], a[44:47], v[192:207]\n"
    "ds_read_b128 v[236:239], v27 offset:43008\n"
    "v_and_b32 v7, 0xffff0000, v145\n"
    "v_and_b32 v8, 0xffff0000, v153\n"
    "v_mul_f32 v162, v5, v6\n"
    "v_mul_f32 v163, v7, v8\n"
    "buffer_load_dword v60, v72, s[48:51], s59 offen offset:1024 sc1\n"
    "s_waitcnt lgkmcnt(6)\n"
    "v_mfma_f32_32x32x16_bf16 v[176:191], v[208:211], a[48:51], v[176:191]\n"
    "ds_read_b64_tr_b16 a[148:149], v93 offset:0\n"
    "ds_read_b64_tr_b16 a[150:151], v93 offset:1024\n"
    "v_mfma_f32_32x32x16_bf16 v[192:207], v[212:215], a[48:51], v[192:207]\n"
    "ds_read_b64_tr_b16 a[144:145], v89 offset:0\n"
    "ds_read_b64_tr_b16 a[146:147], v89 offset:1024\n"
    "s_waitcnt lgkmcnt(8)\n"
    "v_mfma_f32_32x32x16_bf16 v[176:191], v[216:219], a[52:55], v[176:191]\n"
    "ds_read_b64_tr_b16 a[156:157], v93 offset:2048\n"
    "ds_read_b64_tr_b16 a[158:159], v94 offset:3072\n"
    "v_mfma_f32_32x32x16_bf16 v[192:207], v[220:223], a[52:55], v[192:207]\n"
    "ds_read_b64_tr_b16 a[152:153], v89 offset:2048\n"
    "ds_read_b64_tr_b16 a[154:155], v92 offset:3072\n"
    "s_waitcnt lgkmcnt(10)\n"
    "v_mfma_f32_32x32x16_bf16 v[176:191], v[224:227], a[56:59], v[176:191]\n"
    "ds_read_b64_tr_b16 a[164:165], v93 offset:3584\n"
    "ds_read_b64_tr_b16 a[166:167], v93 offset:4608\n"
    "v_mfma_f32_32x32x16_bf16 v[192:207], v[228:231], a[56:59], v[192:207]\n"
    "ds_read_b64_tr_b16 a[160:161], v89 offset:3584\n"
    "ds_read_b64_tr_b16 a[162:163], v89 offset:4608\n"
    "s_waitcnt lgkmcnt(12)\n"
    "v_mfma_f32_32x32x16_bf16 v[176:191], v[232:235], a[60:63], v[176:191]\n"
    "ds_read_b64_tr_b16 a[172:173], v93 offset:5632\n"
    "ds_read_b64_tr_b16 a[174:175], v94 offset:6656\n"
    "v_mfma_f32_32x32x16_bf16 v[192:207], v[236:239], a[60:63], v[192:207]\n"
    "ds_read_b64_tr_b16 a[168:169], v89 offset:5632\n"
    "ds_read_b64_tr_b16 a[170:171], v92 offset:6656\n"
    "v_lshlrev_b32 v5, 16, v148\n"
    "v_lshlrev_b32 v6, 16, v154\n"
    "v_and_b32 v7, 0xffff0000, v148\n"
    "v_and_b32 v8, 0xffff0000, v154\n"
    "v_mul_f32 v164, v5, v6\n"
    "v_mul_f32 v165, v7, v8\n"
    "v_lshlrev_b32 v5, 16, v149\n"
    "v_lshlrev_b32 v6, 16, v155\n"
    "v_and_b32 v7, 0xffff0000, v149\n"
    "v_and_b32 v8, 0xffff0000, v155\n"
    "v_mul_f32 v166, v5, v6\n"
    "v_mul_f32 v167, v7, v8\n"
    "v_lshlrev_b32 v5, 16, v146\n"
    "v_lshlrev_b32 v6, 16, v156\n"
    "v_and_b32 v7, 0xffff0000, v146\n"
    "v_and_b32 v8, 0xffff0000, v156\n"
    "v_mul_f32 v168, v5, v6\n"
    "v_mul_f32 v169, v7, v8\n"
    "v_lshlrev_b32 v5, 16, v147\n"
    "v_lshlrev_b32 v6, 16, v157\n"
    "v_and_b32 v7, 0xffff0000, v147\n"
    "v_and_b32 v8, 0xffff0000, v157\n"
    "v_mul_f32 v170, v5, v6\n"
    "v_mul_f32 v171, v7, v8\n"
    "v_lshlrev_b32 v5, 16, v150\n"
    "v_lshlrev_b32 v6, 16, v158\n"
    "v_and_b32 v7, 0xffff0000, v150\n"
    "v_and_b32 v8, 0xffff0000, v158\n"
    "v_mul_f32 v172, v5, v6\n"
    "v_mul_f32 v173, v7, v8\n"
    "v_lshlrev_b32 v5, 16, v151\n"
    "v_lshlrev_b32 v6, 16, v159\n"
    "v_and_b32 v7, 0xffff0000, v151\n"
    "v_and_b32 v8, 0xffff0000, v159\n"
    "v_mul_f32 v174, v5, v6\n"
    "v_mul_f32 v175, v7, v8\n"
    "s_waitcnt vmcnt(1)\n"
    "s_waitcnt lgkmcnt(0)\n"
    "s_barrier\n"
    "v_add_u32 v14, 57344, v14\n"
    "v_add_u32 v16, 57344, v16\n"
    "v_add_u32 v17, 57344, v17\n"
    "v_add_u32 v20, 57344, v20\n"
    "v_add_u32 v24, 57344, v24\n"
    "v_add_u32 v25, 57344, v25\n"
    "v_add_u32 v26, 57344, v26\n"
    "v_add_u32 v27, 57344, v27\n"
    "ds_read_b128 v[240:243], v14 offset:0\n"
    "ds_read_b128 v[244:247], v24 offset:0\n"
    "ds_read_b128 v[248:251], v16 offset:0\n"
    "ds_read_b128 v[252:255], v25 offset:0\n"
    "ds_read_b128 v[208:211], v17 offset:0\n"
    "ds_read_b128 v[212:215], v26 offset:0\n"
    "ds_read_b128 v[216:219], v20 offset:0\n"
    "ds_read_b128 v[220:223], v27 offset:0\n"
    "s_waitcnt lgkmcnt(6)\n"
    "v_mfma_f32_32x32x16_bf16 v[176:191], v[240:243], a[64:67], v[176:191]\n"
    "ds_read_b128 v[224:227], v14 offset:14336\n"
    "v_mfma_f32_32x32x16_bf16 v[192:207], v[244:247], a[64:67], v[192:207]\n"
    "ds_read_b128 v[228:231], v24 offset:14336\n"
    "s_waitcnt lgkmcnt(6)\n"
    "v_mfma_f32_32x32x16_bf16 v[176:191], v[248:251], a[68:71], v[176:191]\n"
    "ds_read_b128 v[232:235], v16 offset:14336\n"
    "v_mfma_f32_32x32x16_bf16 v[192:207], v[252:255], a[68:71], v[192:207]\n"
    "ds_read_b128 v[236:239], v25 offset:14336\n"
    "s_waitcnt vmcnt(0)\n"
    "v_cmp_eq_u32 vcc, -1, v60\n"
    "s_nop 0\n"
    "s_and_b64 vcc, vcc, -1\n"
    "s_cbranch_scc1 Lprsl_%=\n"
    "Lprok_%=:\n"
    "s_waitcnt lgkmcnt(6)\n"
    "v_mfma_f32_32x32x16_bf16 v[176:191], v[208:211], a[72:75], v[176:191]\n"
    "ds_read_b128 v[240:243], v17 offset:14336\n"
    "global_load_dwordx4 a[112:115], v4, s[40:41] offset:-4096 sc1\n"
    "v_mfma_f32_32x32x16_bf16 v[192:207], v[212:215], a[72:75], v[192:207]\n"
    "ds_read_b128 v[244:247], v26 offset:14336\n"
    "global_load_dwordx4 a[116:119], v4, s[40:41] offset:-3072 sc1\n"
    "s_waitcnt lgkmcnt(6)\n"
    "v_mfma_f32_32x32x16_bf16 v[176:191], v[216:219], a[76:79], v[176:191]\n"
    "ds_read_b128 v[248:251], v20 offset:14336\n"
    "global_load_dwordx4 a[120:123], v4, s[40:41] offset:-2048 sc1\n"
    "v_mfma_f32_32x32x16_bf16 v[192:207], v[220:223], a[76:79], v[192:207]\n"
    "ds_read_b128 v[252:255], v27 offset:14336\n"
    "global_load_dwordx4 a[124:127], v4, s[40:41] offset:-1024 sc1\n"
    "s_waitcnt lgkmcnt(6)\n"
    "v_mfma_f32_32x32x16_bf16 v[176:191], v[224:227], a[80:83], v[176:191]\n"
    "ds_read_b128 v[208:211], v14 offset:28672\n"
    "global_load_dwordx4 a[128:131], v4, s[40:41] offset:0 sc1\n"
    "v_mfma_f32_32x32x16_bf16 v[192:207], v[228:231], a[80:83], v[192:207]\n"
    "ds_read_b128 v[212:215], v24 offset:28672\n"
    "global_load_dwordx4 a[132:135], v4, s[40:41] offset:1024 sc1\n"
    "s_waitcnt lgkmcnt(6)\n"
    "v_mfma_f32_32x32x16_bf16 v[176:191], v[232:235], a[84:87], v[176:191]\n"
    "ds_read_b128 v[216:219], v16 offset:28672\n"
    "global_load_dwordx4 a[136:139], v4, s[40:41] offset:2048 sc1\n"
    "v_mfma_f32_32x32x16_bf16 v[192:207], v[236:239], a[84:87], v[192:207]\n"
    "ds_read_b128 v[220:223], v25 offset:28672\n"
    "global_load_dwordx4 a[140:143], v4, s[40:41] offset:3072 sc1\n"
    "s_waitcnt lgkmcnt(6)\n"
    "v_mfma_f32_32x32x16_bf16 v[176:191], v[240:243], a[88:91], v[176:191]\n"
    "ds_read_b128 v[224:227], v17 offset:28672\n"
    "global_load_dwordx4 a[176:179], v4, s[42:43] offset:-4096 sc1\n"
    "v_mfma_f32_32x32x16_bf16 v[192:207], v[244:247], a[88:91], v[192:207]\n"
    "ds_read_b128 v[228:231], v26 offset:28672\n"
    "global_load_dwordx4 a[180:183], v4, s[42:43] offset:-3072 sc1\n"
    "s_waitcnt lgkmcnt(6)\n"
    "v_mfma_f32_32x32x16_bf16 v[176:191], v[248:251], a[92:95], v[176:191]\n"
    "ds_read_b128 v[232:235], v20 offset:28672\n"
    "global_load_dwordx4 a[184:187], v4, s[42:43] offset:-2048 sc1\n"
    "v_mfma_f32_32x32x16_bf16 v[192:207], v[252:255], a[92:95], v[192:207]\n"
    "ds_read_b128 v[236:239], v27 offset:28672\n"
    "global_load_dwordx4 a[188:191], v4, s[42:43] offset:-1024 sc1\n"
    "s_waitcnt lgkmcnt(6)\n"
    "v_mfma_f32_32x32x16_bf16 v[176:191], v[208:211], a[96:99], v[176:191]\n"
    "global_load_dwordx4 a[208:211], v4, s[42:43] offset:0 sc1\n"
    "v_mfma_f32_32x32x16_bf16 v[192:207], v[212:215], a[96:99], v[192:207]\n"
    "global_load_dwordx4 a[212:215], v4, s[42:43] offset:1024 sc1\n"
    "s_waitcnt lgkmcnt(4)\n"
    "v_mfma_f32_32x32x16_bf16 v[176:191], v[216:219], a[100:103], v[176:191]\n"
    "global_load_dwordx4 a[216:219], v4, s[42:43] offset:2048 sc1\n"
    "v_mfma_f32_32x32x16_bf16 v[192:207], v[220:223], a[100:103], v[192:207]\n"
    "global_load_dwordx4 a[220:223], v4, s[42:43] offset:3072 sc1\n"
    "s_waitcnt lgkmcnt(2)\n"
    "v_mfma_f32_32x32x16_bf16 v[176:191], v[224:227], a[104:107], v[176:191]\n"
    "v_mfma_f32_32x32x16_bf16 v[192:207], v[228:231], a[104:107], v[192:207]\n"
    "s_waitcnt lgkmcnt(0)\n"
    "v_mfma_f32_32x32x16_bf16 v[176:191], v[232:235], a[108:111], v[176:191]\n"
    "v_mfma_f32_32x32x16_bf16 v[192:207], v[236:239], a[108:111], v[192:207]\n"
    "s_waitcnt vmcnt(27)\n"
    "buffer_store_dwordx4 v[30:33], v52, s[48:51], s58 offen sc1\n"
    "buffer_store_dwordx4 v[30:33], v73, s[48:51], 0 offen sc1\n"
    "s_mov_b32 s66, 0\n"
    "s_nop 7\n"
    "v_pk_add_f32 v[176:177], v[176:177], v[160:161]\n"
    "v_pk_add_f32 v[178:179], v[178:179], v[162:163]\n"
    "v_pk_add_f32 v[180:181], v[180:181], v[164:165]\n"
    "v_pk_add_f32 v[182:183], v[182:183], v[166:167]\n"
    "v_pk_add_f32 v[184:185], v[184:185], v[168:169]\n"
    "v_pk_add_f32 v[186:187], v[186:187], v[170:171]\n"
    "v_pk_add_f32 v[188:189], v[188:189], v[172:173]\n"
    "v_pk_add_f32 v[190:191], v[190:191], v[174:175]\n"
    "v_pk_mul_f32 v[192:193], v[116:117], v[192:193] op_sel_hi:[0,1]\n"
    "v_pk_mul_f32 v[194:195], v[116:117], v[194:195] op_sel_hi:[0,1]\n"
    "v_pk_mul_f32 v[196:197], v[116:117], v[196:197] op_sel_hi:[0,1]\n"
    "v_pk_mul_f32 v[198:199], v[116:117], v[198:199] op_sel_hi:[0,1]\n"
    "v_pk_mul_f32 v[200:201], v[116:117], v[200:201] op_sel_hi:[0,1]\n"
    "v_pk_mul_f32 v[202:203], v[116:117], v[202:203] op_sel_hi:[0,1]\n"
    "v_pk_mul_f32 v[204:205], v[116:117], v[204:205] op_sel_hi:[0,1]\n"
    "v_pk_mul_f32 v[206:207], v[116:117], v[206:207] op_sel_hi:[0,1]\n"
    "v_pk_mul_f32 v[176:177], v[116:117], v[176:177] op_sel_hi:[0,1]\n"
    "v_pk_mul_f32 v[178:179], v[116:117], v[178:179] op_sel_hi:[0,1]\n"
    "v_pk_mul_f32 v[180:181], v[116:117], v[180:181] op_sel_hi:[0,1]\n"
    "v_pk_mul_f32 v[182:183], v[116:117], v[182:183] op_sel_hi:[0,1]\n"
    "v_pk_mul_f32 v[184:185], v[116:117], v[184:185] op_sel_hi:[0,1]\n"
    "v_pk_mul_f32 v[186:187], v[116:117], v[186:187] op_sel_hi:[0,1]\n"
    "v_pk_mul_f32 v[188:189], v[116:117], v[188:189] op_sel_hi:[0,1]\n"
    "v_pk_mul_f32 v[190:191], v[116:117], v[190:191] op_sel_hi:[0,1]\n"
    "v_xor_b32 v14, 0, v65\n"
    "v_xor_b32 v16, 32, v65\n"
    "v_xor_b32 v17, 64, v65\n"
    "v_xor_b32 v20, 96, v65\n"
    "v_xor_b32 v24, 128, v65\n"
    "v_xor_b32 v25, 160, v65\n"
    "v_xor_b32 v26, 192, v65\n"
    "v_xor_b32 v27, 224, v65\n"
    "Lwog_%=:\n"
    "s_waitcnt vmcnt(17)\n"
    "v_mfma_f32_32x32x16_bf16 v[128:143], a[148:151], a[112:115], v[192:207]\n"
    "ds_read_b64_tr_b16 a[228:229], v93 offset:7168\n"
    "ds_read_b64_tr_b16 a[230:231], v93 offset:8192\n"
    "s_waitcnt vmcnt(16)\n"
    "v_mfma_f32_32x32x16_bf16 v[128:143], a[156:159], a[116:119], v[128:143]\n"
    "ds_read_b64_tr_b16 a[224:225], v89 offset:7168\n"
    "ds_read_b64_tr_b16 a[226:227], v89 offset:8192\n"
    "v_mfma_f32_32x32x16_bf16 v[144:159], a[144:147], a[112:115], v[176:191]\n"
    "ds_read_b64_tr_b16 a[236:237], v93 offset:9216\n"
    "ds_read_b64_tr_b16 a[238:239], v94 offset:10240\n"
    "s_waitcnt vmcnt(15)\n"
    "v_mfma_f32_32x32x16_bf16 v[128:143], a[164:167], a[120:123], v[128:143]\n"
    "ds_read_b64_tr_b16 a[232:233], v89 offset:9216\n"
    "ds_read_b64_tr_b16 a[234:235], v92 offset:10240\n"
    "v_mfma_f32_32x32x16_bf16 v[144:159], a[152:155], a[116:119], v[144:159]\n"
    "ds_read_b64_tr_b16 a[244:245], v93 offset:10752\n"
    "ds_read_b64_tr_b16 a[246:247], v93 offset:11776\n"
    "s_waitcnt vmcnt(14)\n"
    "v_mfma_f32_32x32x16_bf16 v[128:143], a[172:175], a[124:127], v[128:143]\n"
    "ds_read_b64_tr_b16 a[240:241], v89 offset:10752\n"
    "ds_read_b64_tr_b16 a[242:243], v89 offset:11776\n"
    "v_mfma_f32_32x32x16_bf16 v[144:159], a[160:163], a[120:123], v[144:159]\n"
    "ds_read_b64_tr_b16 a[252:253], v93 offset:12800\n"
    "ds_read_b64_tr_b16 a[254:255], v94 offset:13824\n"
    "s_waitcnt vmcnt(13)\n"
    "s_waitcnt lgkmcnt(10)\n"
    "v_mfma_f32_32x32x16_bf16 v[128:143], a[228:231], a[128:131], v[128:143]\n"
    "ds_read_b64_tr_b16 a[248:249], v89 offset:12800\n"
    "ds_read_b64_tr_b16 a[250:251], v92 offset:13824\n"
    "v_mfma_f32_32x32x16_bf16 v[144:159], a[168:171], a[124:127], v[144:159]\n"
    "ds_read_b64_tr_b16 a[4:5], v93 offset:14336\n"
    "ds_read_b64_tr_b16 a[6:7], v93 offset:15360\n"
    "s_waitcnt vmcnt(12)\n"
    "s_waitcnt lgkmcnt(10)\n"
    "v_mfma_f32_32x32x16_bf16 v[128:143], a[236:239], a[132:135], v[128:143]\n"
    "ds_read_b64_tr_b16 a[0:1], v89 offset:14336\n"
    "ds_read_b64_tr_b16 a[2:3], v89 offset:15360\n"
    "v_mfma_f32_32x32x16_bf16 v[144:159], a[224:227], a[128:131], v[144:159]\n"
    "ds_read_b64_tr_b16 a[12:13], v93 offset:16384\n"
    "ds_read_b64_tr_b16 a[14:15], v94 offset:17408\n"
    "s_waitcnt vmcnt(11)\n"
    "s_waitcnt lgkmcnt(10)\n"
    "v_mfma_f32_32x32x16_bf16 v[128:143], a[244:247], a[136:139], v[128:143]\n"
    "ds_read_b64_tr_b16 a[8:9], v89 offset:16384\n"
    "ds_read_b64_tr_b16 a[10:11], v92 offset:17408\n"
    "v_mfma_f32_32x32x16_bf16 v[144:159], a[232:235], a[132:135], v[144:159]\n"
    "ds_read_b64_tr_b16 a[20:21], v93 offset:17920\n"
    "ds_read_b64_tr_b16 a[22:23], v93 offset:18944\n"
    "s_waitcnt vmcnt(10)\n"
    "s_waitcnt lgkmcnt(10)\n"
    "v_mfma_f32_32x32x16_bf16 v[128:143], a[252:255], a[140:143], v[128:143]\n"
    "ds_read_b64_tr_b16 a[16:17], v89 offset:17920\n"
    "ds_read_b64_tr_b16 a[18:19], v89 offset:18944\n"
    "v_mfma_f32_32x32x16_bf16 v[144:159], a[240:243], a[136:139], v[144:159]\n"
    "ds_read_b64_tr_b16 a[28:29], v93 offset:19968\n"
    "ds_read_b64_tr_b16 a[30:31], v94 offset:20992\n"
    "s_waitcnt vmcnt(9)\n"
    "s_waitcnt lgkmcnt(10)\n"
    "v_mfma_f32_32x32x16_bf16 v[128:143], a[4:7], a[176:179], v[128:143]\n"
    "ds_read_b64_tr_b16 a[24:25], v89 offset:19968\n"
    "ds_read_b64_tr_b16 a[26:27], v92 offset:20992\n"
    "v_mfma_f32_32x32x16_bf16 v[144:159], a[248:251], a[140:143], v[144:159]\n"
    "ds_read_b64_tr_b16 a[36:37], v93 offset:21504\n"
    "ds_read_b64_tr_b16 a[38:39], v93 offset:22528\n"
    "s_waitcnt vmcnt(8)\n"
    "s_waitcnt lgkmcnt(10)\n"
    "v_mfma_f32_32x32x16_bf16 v[128:143], a[12:15], a[180:183], v[128:143]\n"
    "ds_read_b64_tr_b16 a[32:33], v89 offset:21504\n"
    "ds_read_b64_tr_b16 a[34:35], v89 offset:22528\n"
    "v_mfma_f32_32x32x16_bf16 v[144:159], a[0:3], a[176:179], v[144:159]\n"
    "ds_read_b64_tr_b16 a[44:45], v93 offset:23552\n"
    "ds_read_b64_tr_b16 a[46:47], v94 offset:24576\n"
    "s_waitcnt vmcnt(7)\n"
    "s_waitcnt lgkmcnt(10)\n"
    "v_mfma_f32_32x32x16_bf16 v[128:143], a[20:23], a[184:187], v[128:143]\n"
    "ds_read_b64_tr_b16 a[40:41], v89 offset:23552\n"
    "ds_read_b64_tr_b16 a[42:43], v92 offset:24576\n"
    "v_mfma_f32_32x32x16_bf16 v[144:159], a[8:11], a[180:183], v[144:159]\n"
    "ds_read_b64_tr_b16 a[52:53], v93 offset:25088\n"
    "ds_read_b64_tr_b16 a[54:55], v93 offset:26112\n"
    "s_waitcnt vmcnt(6)\n"
    "s_waitcnt lgkmcnt(10)\n"
    "v_mfma_f32_32x32x16_bf16 v[128:143], a[28:31], a[188:191], v[128:143]\n"
    "ds_read_b64_tr_b16 a[48:49], v89 offset:25088\n"
    "ds_read_b64_tr_b16 a[50:51], v89 offset:26112\n"
    "v_mfma_f32_32x32x16_bf16 v[144:159], a[16:19], a[184:187], v[144:159]\n"
    "ds_read_b64_tr_b16 a[60:61], v93 offset:27136\n"
    "ds_read_b64_tr_b16 a[62:63], v94 offset:28160\n"
    "s_waitcnt vmcnt(5)\n"
    "s_waitcnt lgkmcnt(10)\n"
    "v_mfma_f32_32x32x16_bf16 v[128:143], a[36:39], a[208:211], v[128:143]\n"
    "ds_read_b64_tr_b16 a[56:57], v89 offset:27136\n"
    "ds_read_b64_tr_b16 a[58:59], v92 offset:28160\n"
    "v_mfma_f32_32x32x16_bf16 v[144:159], a[24:27], a[188:191], v[144:159]\n"
    "s_waitcnt vmcnt(4)\n"
    "s_waitcnt lgkmcnt(8)\n"
    "v_mfma_f32_32x32x16_bf16 v[128:143], a[44:47], a[212:215], v[128:143]\n"
    "v_mfma_f32_32x32x16_bf16 v[144:159], a[32:35], a[208:211], v[144:159]\n"
    "s_waitcnt vmcnt(3)\n"
    "s_waitcnt lgkmcnt(4)\n"
    "v_mfma_f32_32x32x16_bf16 v[128:143], a[52:55], a[216:219], v[128:143]\n"
    "v_mfma_f32_32x32x16_bf16 v[144:159], a[40:43], a[212:215], v[144:159]\n"
    "s_waitcnt vmcnt(2)\n"
    "s_waitcnt lgkmcnt(0)\n"
    "v_mfma_f32_32x32x16_bf16 v[128:143], a[60:63], a[220:223], v[128:143]\n"
    "v_mfma_f32_32x32x16_bf16 v[144:159], a[48:51], a[216:219], v[144:159]\n"
    "v_mfma_f32_32x32x16_bf16 v[144:159], a[56:59], a[220:223], v[144:159]\n"
    "s_nop 8\n"
    "s_cmp_eq_u32 s66, 0\n"
    "v_cmp_u_f32 vcc, v128, v128\n"
    "s_cselect_b64 s[80:81], -1, 0\n"
    "s_and_b64 vcc, vcc, s[80:81]\n"
    "s_cbranch_scc1 Lredo_%=\n"
    "ds_write_b128 v24, v[128:131] offset:0\n"
    "ds_write_b128 v25, v[132:135] offset:0\n"
    "ds_write_b128 v26, v[136:139] offset:0\n"
    "ds_write_b128 v27, v[140:143] offset:0\n"
    "ds_write_b128 v14, v[144:147] offset:0\n"
    "ds_write_b128 v16, v[148:151] offset:0\n"
    "ds_write_b128 v17, v[152:155] offset:0\n"
    "ds_write_b128 v20, v[156:159] offset:0\n"
    "s_waitcnt lgkmcnt(0)\n"
    "s_barrier\n"
    "ds_read_b128 v[208:211], v74 offset:0\n"
    "ds_read_b128 v[212:215], v74 offset:16384\n"
    "ds_read_b128 v[216:219], v64 offset:1024\n"
    "ds_read_b128 v[220:223], v64 offset:17408\n"
    "ds_read_b128 v[224:227], v74 offset:2048\n"
    "ds_read_b128 v[228:231], v74 offset:18432\n"
    "ds_read_b128 v[232:235], v64 offset:3072\n"
    "ds_read_b128 v[236:239], v64 offset:19456\n"
    "s_waitcnt lgkmcnt(6)\n"
    "v_pk_add_f32 v[208:209], v[208:209], v[212:213]\n"
    "v_pk_add_f32 v[210:211], v[210:211], v[214:215]\n"
    "buffer_store_dwordx4 v[208:211], v69, s[52:55], s39 offen offset:0\n"
    "s_waitcnt lgkmcnt(4)\n"
    "v_pk_add_f32 v[216:217], v[216:217], v[220:221]\n"
    "v_pk_add_f32 v[218:219], v[218:219], v[222:223]\n"
    "buffer_store_dwordx4 v[216:219], v69, s[52:55], s44 offen offset:0\n"
    "s_waitcnt lgkmcnt(2)\n"
    "v_pk_add_f32 v[224:225], v[224:225], v[228:229]\n"
    "v_pk_add_f32 v[226:227], v[226:227], v[230:231]\n"
    "buffer_store_dwordx4 v[224:227], v69, s[52:55], s39 offen offset:3584\n"
    "s_waitcnt lgkmcnt(0)\n"
    "v_pk_add_f32 v[232:233], v[232:233], v[236:237]\n"
    "v_pk_add_f32 v[234:235], v[234:235], v[238:239]\n"
    "buffer_store_dwordx4 v[232:235], v69, s[52:55], s44 offen offset:3584\n"
    "s_endpgm\n"
    "Lredo_%=:\n"
    "s_mov_b32 s66, 1\n"
    "s_nop 7\n"
    "buffer_load_dwordx2 v[60:61], v57, s[48:51], s56 offen sc1\n"
    "s_waitcnt vmcnt(0)\n"
    "v_cmp_ne_u64 vcc, s[78:79], v[60:61]\n"
    "s_nop 0\n"
    "s_and_b64 vcc, vcc, 0xff\n"
    "s_cbranch_scc1 Lfosl_%=\n"
    "Lfook_%=:\n"
    "global_load_dwordx4 a[112:115], v4, s[40:41] offset:-4096 sc1\n"
    "global_load_dwordx4 a[116:119], v4, s[40:41] offset:-3072 sc1\n"
    "global_load_dwordx4 a[120:123], v4, s[40:41] offset:-2048 sc1\n"
    "global_load_dwordx4 a[124:127], v4, s[40:41] offset:-1024 sc1\n"
    "global_load_dwordx4 a[128:131], v4, s[40:41] offset:0 sc1\n"
    "global_load_dwordx4 a[132:135], v4, s[40:41] offset:1024 sc1\n"
    "global_load_dwordx4 a[136:139], v4, s[40:41] offset:2048 sc1\n"
    "global_load_dwordx4 a[140:143], v4, s[40:41] offset:3072 sc1\n"
    "global_load_dwordx4 a[176:179], v4, s[42:43] offset:-4096 sc1\n"
    "global_load_dwordx4 a[180:183], v4, s[42:43] offset:-3072 sc1\n"
    "global_load_dwordx4 a[184:187], v4, s[42:43] offset:-2048 sc1\n"
    "global_load_dwordx4 a[188:191], v4, s[42:43] offset:-1024 sc1\n"
    "global_load_dwordx4 a[208:211], v4, s[42:43] offset:0 sc1\n"
    "global_load_dwordx4 a[212:215], v4, s[42:43] offset:1024 sc1\n"
    "global_load_dwordx4 a[216:219], v4, s[42:43] offset:2048 sc1\n"
    "global_load_dwordx4 a[220:223], v4, s[42:43] offset:3072 sc1\n"
    "s_waitcnt vmcnt(0) lgkmcnt(0)\n"
    "s_branch Lwog_%=\n"
    "Lfb_%=:\n"
    "s_waitcnt vmcnt(0)\n"
    "v_mov_b32 v7, 0xff800000\n"
    "s_mov_b32 s87, 262144\n"
    "s_mov_b32 s86, 64\n"
    "Lfb1_%=:\n"
    "buffer_load_dwordx4 v[208:211], v4, s[48:51], s87 offen offset:0 sc1\n"
    "buffer_load_dwordx4 v[212:215], v4, s[48:51], s87 offen offset:1024 sc1\n"
    "s_waitcnt vmcnt(0)\n"
    "v_mfma_f32_32x32x16_bf16 v[176:191], v[208:211], a[144:147], 0\n"
    "v_mfma_f32_32x32x16_bf16 v[176:191], v[212:215], a[148:151], v[176:191]\n"
    "s_add_u32 s87, s87, 4096\n"
    "s_sub_u32 s86, s86, 1\n"
    "s_cmp_lg_u32 s86, 0\n"
    "s_nop 9\n"
    "v_max3_f32 v7, v7, v176, v177\n"
    "v_max3_f32 v7, v7, v178, v179\n"
    "v_max3_f32 v7, v7, v180, v181\n"
    "v_max3_f32 v7, v7, v182, v183\n"
    "v_max3_f32 v7, v7, v184, v185\n"
    "v_max3_f32 v7, v7, v186, v187\n"
    "v_max3_f32 v7, v7, v188, v189\n"
    "v_max3_f32 v7, v7, v190, v191\n"
    "s_cbranch_scc1 Lfb1_%=\n"
    "v_mov_b32 v8, v7\n"
    "s_nop 1\n"
    "v_permlane32_swap_b32 v7, v8\n"
    "s_nop 1\n"
    "v_max_f32 v7, v7, v8\n"
    "v_sub_f32 v224, 0, v7\n"
    "v_mov_b32 v160, 0\n"
    "v_sub_f32 v225, 0, v7\n"
    "v_mov_b32 v161, 0\n"
    "v_sub_f32 v226, 0, v7\n"
    "v_mov_b32 v162, 0\n"
    "v_sub_f32 v227, 0, v7\n"
    "v_mov_b32 v163, 0\n"
    "v_sub_f32 v228, 0, v7\n"
    "v_mov_b32 v164, 0\n"
    "v_sub_f32 v229, 0, v7\n"
    "v_mov_b32 v165, 0\n"
    "v_sub_f32 v230, 0, v7\n"
    "v_mov_b32 v166, 0\n"
    "v_sub_f32 v231, 0, v7\n"
    "v_mov_b32 v167, 0\n"
    "v_sub_f32 v232, 0, v7\n"
    "v_mov_b32 v168, 0\n"
    "v_sub_f32 v233, 0, v7\n"
    "v_mov_b32 v169, 0\n"
    "v_sub_f32 v234, 0, v7\n"
    "v_mov_b32 v170, 0\n"
    "v_sub_f32 v235, 0, v7\n"
    "v_mov_b32 v171, 0\n"
    "v_sub_f32 v236, 0, v7\n"
    "v_mov_b32 v172, 0\n"
    "v_sub_f32 v237, 0, v7\n"
    "v_mov_b32 v173, 0\n"
    "v_sub_f32 v238, 0, v7\n"
    "v_mov_b32 v174, 0\n"
    "v_sub_f32 v239, 0, v7\n"
    "v_mov_b32 v175, 0\n"
    "s_mov_b32 s87, 262144\n"
    "s_mov_b32 s86, 64\n"
    "Lfb2_%=:\n"
    "buffer_load_dwordx4 v[208:211], v4, s[48:51], s87 offen offset:0 sc1\n"
    "buffer_load_dwordx4 v[212:215], v4, s[48:51], s87 offen offset:1024 sc1\n"
    "buffer_load_dwordx4 v[216:219], v4, s[48:51], s87 offen offset:2048 sc1\n"
    "buffer_load_dwordx4 v[220:223], v4, s[48:51], s87 offen offset:3072 sc1\n"
    "s_waitcnt vmcnt(0)\n"
    "v_mfma_f32_32x32x16_bf16 v[176:191], v[208:211], a[144:147], v[224:239]\n"
    "v_mfma_f32_32x32x16_bf16 v[176:191], v[212:215], a[148:151], v[176:191]\n"
    "s_add_u32 s87, s87, 4096\n"
    "s_sub_u32 s86, s86, 1\n"
    "s_cmp_lg_u32 s86, 0\n"
    "s_nop 9\n"
    "v_exp_f32 v176, v176\n"
    "v_exp_f32 v177, v177\n"
    "v_exp_f32 v178, v178\n"
    "v_exp_f32 v179, v179\n"
    "v_exp_f32 v180, v180\n"
    "v_exp_f32 v181, v181\n"
    "v_exp_f32 v182, v182\n"
    "v_exp_f32 v183, v183\n"
    "v_exp_f32 v184, v184\n"
    "v_exp_f32 v185, v185\n"
    "v_exp_f32 v186, v186\n"
    "v_exp_f32 v187, v187\n"
    "v_exp_f32 v188, v188\n"
    "v_exp_f32 v189, v189\n"
    "v_exp_f32 v190, v190\n"
    "v_exp_f32 v191, v191\n"
    "s_nop 0\n"
    "v_cvt_pk_bf16_f32 v120, v176, v177\n"
    "v_cvt_pk_bf16_f32 v121, v178, v179\n"
    "v_cvt_pk_bf16_f32 v122, v180, v181\n"
    "v_cvt_pk_bf16_f32 v123, v182, v183\n"
    "v_cvt_pk_bf16_f32 v124, v184, v185\n"
    "v_cvt_pk_bf16_f32 v125, v186, v187\n"
    "v_cvt_pk_bf16_f32 v126, v188, v189\n"
    "v_cvt_pk_bf16_f32 v127, v190, v191\n"
    "s_nop 1\n"
    "v_mfma_f32_32x32x16_bf16 v[160:175], v[216:219], v[120:123], v[160:175]\n"
    "v_mfma_f32_32x32x16_bf16 v[160:175], v[220:223], v[124:127], v[160:175]\n"
    "s_nop 11\n"
    "s_cbranch_scc1 Lfb2_%=\n"
    "v_mov_b32 v18, v172\n"
    "v_mov_b32 v5, v172\n"
    "s_nop 1\n"
    "v_permlane32_swap_b32 v18, v5\n"
    "s_nop 1\n"
    "v_rcp_f32 v18, v5\n"
    "s_nop 0\n"
    "v_mul_f32 v18, v18, v15\n"
    "s_branch Lnorm2_%=\n"
    "Lkvsl_%=:\n"
    "s_mov_b32 s86, 262144\n"
    "Lkvsp_%=:\n"
    "buffer_load_dwordx2 v[60:61], v58, s[48:51], s87 offen sc1\n"
    "s_waitcnt vmcnt(0)\n"
    "v_cmp_ne_u64 vcc, s[78:79], v[60:61]\n"
    "s_sub_u32 s86, s86, 1\n"
    "s_cbranch_scc1 Lkvok_%=\n"
    "s_and_b64 vcc, vcc, -1\n"
    "s_cbranch_scc1 Lkvsp_%=\n"
    "s_branch Lkvok_%=\n"
    "Lfesl_%=:\n"
    "s_mov_b32 s86, 262144\n"
    "Lfesp_%=:\n"
    "buffer_load_dwordx2 v[60:61], v95, s[48:51], s39 offen sc1\n"
    "s_waitcnt vmcnt(0)\n"
    "v_cmp_ne_u64 vcc, s[78:79], v[60:61]\n"
    "s_sub_u32 s86, s86, 1\n"
    "s_cbranch_scc1 Lfeok_%=\n"
    "s_and_b64 vcc, vcc, s[80:81]\n"
    "s_cbranch_scc1 Lfesp_%=\n"
    "s_branch Lfeok_%=\n"
    "Lfhsl_%=:\n"
    "s_mov_b32 s86, 262144\n"
    "Lfhsp_%=:\n"
    "buffer_load_dwordx2 v[60:61], v96, s[48:51], s64 offen sc1\n"
    "s_waitcnt vmcnt(0)\n"
    "v_cmp_ne_u64 vcc, s[78:79], v[60:61]\n"
    "s_sub_u32 s86, s86, 1\n"
    "s_cbranch_scc1 Lfhok_%=\n"
    "s_and_b64 vcc, vcc, s[80:81]\n"
    "s_cbranch_scc1 Lfhsp_%=\n"
    "s_branch Lfhok_%=\n"
    "Lprsl_%=:\n"
    "s_mov_b32 s86, 6\n"
    "Lprsp_%=:\n"
    "buffer_load_dword v60, v72, s[48:51], s59 offen offset:1024 sc1\n"
    "s_waitcnt vmcnt(0)\n"
    "v_cmp_eq_u32 vcc, -1, v60\n"
    "s_sub_u32 s86, s86, 1\n"
    "s_cbranch_scc1 Lprok_%=\n"
    "s_and_b64 vcc, vcc, -1\n"
    "s_cbranch_scc1 Lprsp_%=\n"
    "s_branch Lprok_%=\n"
    "Lfosl_%=:\n"
    "s_mov_b32 s86, 262144\n"
    "Lfosp_%=:\n"
    "buffer_load_dwordx2 v[60:61], v57, s[48:51], s56 offen sc1\n"
    "s_waitcnt vmcnt(0)\n"
    "v_cmp_ne_u64 vcc, s[78:79], v[60:61]\n"
    "s_sub_u32 s86, s86, 1\n"
    "s_cbranch_scc1 Lfook_%=\n"
    "s_and_b64 vcc, vcc, 0xff\n"
    "s_cbranch_scc1 Lfosp_%=\n"
    "s_branch Lfook_%=\n"
    "Lend_%=:\n"
    :
    : "s"(ka), "s"(bid), "v"(tid), "s"(ldsbase)
    :
      "v1","v2","v3","v4","v5","v6","v7","v8","v9","v10","v11","v12","v13","v14","v15","v16","v17","v18","v19","v20","v21","v22","v23","v24",
      "v25","v26","v27","v28","v29","v30","v31","v32","v33","v34","v35","v36","v37","v38","v39","v40","v41","v42","v43","v44","v45","v46","v47","v48",
      "v49","v50","v51","v52","v53","v54","v55","v56","v57","v58","v59","v60","v61","v62","v63","v64","v65","v66","v67","v68","v69","v70","v71","v72",
      "v73","v74","v75","v76","v77","v78","v79","v80","v81","v82","v83","v84","v85","v86","v87","v88","v89","v90","v91","v92","v93","v94","v95","v96",
      "v97","v98","v99","v100","v101","v102","v103","v104","v105","v106","v107","v108","v109","v110","v111","v112","v113","v114","v115","v116","v117","v118","v119","v120",
      "v121","v122","v123","v124","v125","v126","v127","v128","v129","v130","v131","v132","v133","v134","v135","v136","v137","v138","v139","v140","v141","v142","v143","v144",
      "v145","v146","v147","v148","v149","v150","v151","v152","v153","v154","v155","v156","v157","v158","v159","v160","v161","v162","v163","v164","v165","v166","v167","v168",
      "v169","v170","v171","v172","v173","v174","v175","v176","v177","v178","v179","v180","v181","v182","v183","v184","v185","v186","v187","v188","v189","v190","v191","v192",
      "v193","v194","v195","v196","v197","v198","v199","v200","v201","v202","v203","v204","v205","v206","v207","v208","v209","v210","v211","v212","v213","v214","v215","v216",
      "v217","v218","v219","v220","v221","v222","v223","v224","v225","v226","v227","v228","v229","v230","v231","v232","v233","v234","v235","v236","v237","v238","v239","v240",
      "v241","v242","v243","v244","v245","v246","v247","v248","v249","v250","v251","v252","v253","v254","v255","a0","a1","a2","a3","a4","a5","a6","a7","a8",
      "a9","a10","a11","a12","a13","a14","a15","a16","a17","a18","a19","a20","a21","a22","a23","a24","a25","a26","a27","a28","a29","a30","a31","a32",
      "a33","a34","a35","a36","a37","a38","a39","a40","a41","a42","a43","a44","a45","a46","a47","a48","a49","a50","a51","a52","a53","a54","a55","a56",
      "a57","a58","a59","a60","a61","a62","a63","a64","a65","a66","a67","a68","a69","a70","a71","a72","a73","a74","a75","a76","a77","a78","a79","a80",
      "a81","a82","a83","a84","a85","a86","a87","a88","a89","a90","a91","a92","a93","a94","a95","a96","a97","a98","a99","a100","a101","a102","a103","a104",
      "a105","a106","a107","a108","a109","a110","a111","a112","a113","a114","a115","a116","a117","a118","a119","a120","a121","a122","a123","a124","a125","a126","a127","a128",
      "a129","a130","a131","a132","a133","a134","a135","a136","a137","a138","a139","a140","a141","a142","a143","a144","a145","a146","a147","a148","a149","a150","a151","a152",
      "a153","a154","a155","a156","a157","a158","a159","a160","a161","a162","a163","a164","a165","a166","a167","a168","a169","a170","a171","a172","a173","a174","a175","a176",
      "a177","a178","a179","a180","a181","a182","a183","a184","a185","a186","a187","a188","a189","a190","a191","a192","a193","a194","a195","a196","a197","a198","a199","a200",
      "a201","a202","a203","a204","a205","a206","a207","a208","a209","a210","a211","a212","a213","a214","a215","a216","a217","a218","a219","a220","a221","a222","a223","a224",
      "a225","a226","a227","a228","a229","a230","a231","a232","a233","a234","a235","a236","a237","a238","a239","a240","a241","a242","a243","a244","a245","a246","a247","a248",
      "a249","a250","a251","a252","a253","a254","a255","s4","s5","s6","s7","s8","s9","s10","s11","s12","s13","s14","s15","s16","s17","s18","s19","s20",
      "s21","s22","s23","s24","s25","s26","s27","s28","s29","s30","s31","s33","s34","s35","s36","s37","s38","s39","s40","s41","s42","s43","s44","s45",
      "s46","s47","s48","s49","s50","s51","s52","s53","s54","s55","s56","s57","s58","s59","s60","s61","s62","s63","s64","s65","s66","s67","s68","s69",
      "s70","s71","s72","s73","s74","s75","s76","s77","s78","s79","s80","s81","s82","s83","s84","s85","s86","s87","s88","s89","s90","s91","s92","s93",
      "s94","s95","s96","s97","s98","s99","vcc","scc","memory"
  );
}

extern "C" void kernel_launch(void* const* d_in, const int* in_sizes, int n_in, void* d_out, int out_size,
                              void* d_ws, size_t ws_size, hipStream_t stream) {
  const int idx[16] = {0, 1, 2, 3, 8, 9, 10, 11, 4, 14, -1, -2, 6, 7, 12, 13};
  Args a;
  for (int i = 0; i < 16; ++i) {
    if (idx[i] >= 0) { a.b[i].p = d_in[idx[i]]; a.b[i].bytes = (uint32_t)in_sizes[idx[i]] * 2u; }
    else if (idx[i] == -1) { a.b[i].p = d_ws; a.b[i].bytes = 9437184u; }
    else { a.b[i].p = d_out; a.b[i].bytes = (uint32_t)out_size * 4u; }
    a.b[i].fmt = 0x00020000u;
  }
  gated_mix_fused_kernel<<<256, 256, 0, stream>>>(a);
}
```
